```python
import torch

def _softmax(x, axis=-1):
    return torch.softmax(x, dim=axis)
def _gelu(x):
    return torch.nn.functional.gelu(x, approximate="tanh")
def _silu(x):
    return torch.nn.functional.silu(x)
def _sigmoid(x):
    return torch.sigmoid(x)
def _layernorm(x, axis=-1, eps=1e-5):
    m = x.mean(dim=axis, keepdim=True)
    v = x.var(dim=axis, keepdim=True, unbiased=False)
    return (x - m) / torch.sqrt(v + eps)
def _scatter_add(tgt, idx, src, axis=0):
    return tgt.clone().index_add_(axis, idx, src)
def _segment_sum(src, seg, n):
    out = torch.zeros((n,) + src.shape[1:], dtype=src.dtype)
    return out.index_add_(0, seg, src)
def _pad(x, pw):
    flat = tuple(v for pair in reversed(pw) for v in pair)
    return torch.nn.functional.pad(x, flat)

kernel_name = 'standalone__detection_seg_fpn_lateral__vision'

def setup_inputs():

    _w = (lambda a: (lambda t: t.to(torch.bfloat16) if t.is_floating_point() else t)(torch.as_tensor(a)))
    H = W = (384 // 2) * 2
    C = 96
    k = 3
    return {
        "c_fine": _w((lambda *s: torch.randn(*s, dtype=torch.bfloat16) * 0.05)(1, H, W, C)),
        "p_coarse": _w((lambda *s: torch.randn(*s, dtype=torch.bfloat16) * 0.05)(1, H // 2, W // 2, C)),
        "w_lat": _w((lambda *s: torch.randn(*s, dtype=torch.bfloat16) * 0.05)(C, C)),
        "w_out": _w((lambda *s: torch.randn(*s, dtype=torch.bfloat16) * 0.05)(k, k, C, C)),
    }

def reference(c_fine, p_coarse, w_lat, w_out):
    B, H, W, C = c_fine.shape
    k = 3
    lat = torch.einsum("bhwc,cd->bhwd", c_fine, w_lat)
    up = (lambda x, n, axis: torch.repeat_interleave(x, n, dim=axis))((lambda x, n, axis: torch.repeat_interleave(x, n, dim=axis))(p_coarse, 2, axis=1), 2, axis=2)
    m = lat + up
    pad = k // 2
    mp = _pad(m, ((0, 0), (pad, pad), (pad, pad), (0, 0)))
    o = None
    for di in range(k):
        for dj in range(k):
            t = torch.einsum("bhwc,cd->bhwd", mp[:, di:di + H, dj:dj + W, :], w_out[di, dj])
            o = t if o is None else o + t
    return o
```

```cpp
#include <hip/hip_runtime.h>
#include <hip/hip_bf16.h>

typedef __bf16 bf16x8 __attribute__((ext_vector_type(8)));
typedef __bf16 bf16x4 __attribute__((ext_vector_type(4)));
typedef __bf16 bf16x2 __attribute__((ext_vector_type(2)));
typedef float f32x4 __attribute__((ext_vector_type(4)));
typedef float f32x2 __attribute__((ext_vector_type(2)));
typedef unsigned u32x4 __attribute__((ext_vector_type(4)));
typedef unsigned u32x2 __attribute__((ext_vector_type(2)));
typedef __attribute__((address_space(3))) bf16x4* lds_bf16x4_p;

#define DI __device__ __forceinline__

constexpr int HH = 384, WW = 384, CC = 96;
constexpr int TW = 48, TH = 12;
constexpr int PXB = 192;
constexpr int MROW = 50 * PXB;
constexpr int MBYTES = 14 * MROW;
constexpr int LDS_BYTES = 163840;

DI unsigned cvtpk(float lo, float hi) {
  f32x2 v = {lo, hi}; bf16x2 b = __builtin_convertvector(v, bf16x2); return __builtin_bit_cast(unsigned, b);
}
DI float bflo(unsigned u) { return __uint_as_float(u << 16); }
DI float bfhi(unsigned u) { return __uint_as_float(u & 0xffff0000u); }

DI bf16x8 tr_frag(char* smem, unsigned addr0) {
  bf16x4 a = __builtin_amdgcn_ds_read_tr16_b64_v4bf16((lds_bf16x4_p)(smem + addr0));
  bf16x4 b = __builtin_amdgcn_ds_read_tr16_b64_v4bf16((lds_bf16x4_p)(smem + addr0 + 4 * PXB));
  return __builtin_shufflevector(a, b, 0, 1, 2, 3, 4, 5, 6, 7);
}

__global__ __launch_bounds__(256, 1)
void fpn_kernel(const __bf16* __restrict__ cf, const __bf16* __restrict__ pc,
                const __bf16* __restrict__ wlat, const __bf16* __restrict__ wout,
                __bf16* __restrict__ out) {
  __shared__ __attribute__((aligned(16))) char smem[LDS_BYTES];
  const int tid = threadIdx.x;
  const int lane = tid & 63;
  const int wid = __builtin_amdgcn_readfirstlane(tid >> 6);
  const int c = wid & 1, g = wid >> 1;
  const int i16 = lane & 15, g4 = lane >> 4;
  const int bs4 = ((g4 & 1) << 1) | (g4 >> 1);
  const int tx = blockIdx.x & 7, ty = blockIdx.x >> 3;
  const int x0 = tx * TW, y0 = ty * TH;

  bf16x8 wo[3][3][3][3];
  bf16x8 wl[3][3];
  const int q = i16 >> 2, p = i16 & 3;
  const unsigned trA = (8 * bs4 + q) * PXB + (48 * c + 8 * p) * 2;
  const unsigned trB = (8 * bs4 + q) * PXB + (48 * c + 32 + 4 * p) * 2;
  {
    for (int k = 0; k < 27; ++k) {
      int ch = k * 256 + tid;
      u32x4 v;
      if (ch < 1152) v = *(const u32x4*)((const char*)wlat + ch * 16);
      else v = *(const u32x4*)((const char*)wout + (ch - 1152) * 16);
      *(u32x4*)(smem + ch * 16) = v;
    }
    __syncthreads();
#pragma unroll
    for (int ks = 0; ks < 3; ++ks) {
      wl[ks][0] = tr_frag(smem, trA + ks * 6144);
      wl[ks][1] = tr_frag(smem, trA + 8 + ks * 6144);
      wl[ks][2] = tr_frag(smem, trB + ks * 6144);
    }
#pragma unroll
    for (int t = 0; t < 5; ++t)
#pragma unroll
      for (int ks = 0; ks < 3; ++ks) {
        unsigned b = 18432 * (t + 1) + ks * 6144;
        wo[t / 3][t % 3][ks][0] = tr_frag(smem, trA + b);
        wo[t / 3][t % 3][ks][1] = tr_frag(smem, trA + 8 + b);
        wo[t / 3][t % 3][ks][2] = tr_frag(smem, trB + b);
      }
    __syncthreads();
    for (int k = 0; k < 18; ++k) {
      int ch = k * 256 + tid;
      u32x4 v = *(const u32x4*)((const char*)wout + 5 * 18432 + ch * 16);
      *(u32x4*)(smem + ch * 16) = v;
    }
    __syncthreads();
#pragma unroll
    for (int t = 5; t < 9; ++t)
#pragma unroll
      for (int ks = 0; ks < 3; ++ks) {
        unsigned b = 18432 * (t - 5) + ks * 6144;
        wo[t / 3][t % 3][ks][0] = tr_frag(smem, trA + b);
        wo[t / 3][t % 3][ks][1] = tr_frag(smem, trA + 8 + b);
        wo[t / 3][t % 3][ks][2] = tr_frag(smem, trB + b);
      }
    __syncthreads();
  }

  __amdgpu_buffer_rsrc_t rcf = __builtin_amdgcn_make_buffer_rsrc((void*)cf, 0, HH * WW * PXB, 0x00020000);
  __amdgpu_buffer_rsrc_t rpc = __builtin_amdgcn_make_buffer_rsrc((void*)pc, 0, (HH / 2) * (WW / 2) * PXB, 0x00020000);
  for (int it = 0; it < 22; ++it) {
    int grp = it * 2 + g;
    int ry, cx;
    bool ok = true;
    if (grp < 2) { int idx = grp * 16 + i16; ok = idx < 28; ry = idx >> 1; cx = idx & 1; if (!ok) { ry = 0; } }
    else { int gg = grp - 2; ry = gg / 3; cx = 2 + (gg % 3) * 16 + i16; }
    int gy = y0 - 1 + ry, gx = x0 - 1 + cx;
    bool valid = ok && gy >= 0 && gy < HH && gx >= 0 && gx < WW;
    unsigned vcf = valid ? (unsigned)((gy * WW + gx) * PXB + bs4 * 16) : 0x80000000u;
    unsigned vpc = valid ? (unsigned)(((gy >> 1) * (WW / 2) + (gx >> 1)) * PXB + 96 * c) : 0x80000000u;
    u32x4 b0 = __builtin_amdgcn_raw_buffer_load_b128(rcf, vcf, 0, 0);
    u32x4 b1 = __builtin_amdgcn_raw_buffer_load_b128(rcf, vcf + 64, 0, 0);
    u32x4 b2 = __builtin_amdgcn_raw_buffer_load_b128(rcf, vcf + 128, 0, 0);
    u32x4 u0 = __builtin_amdgcn_raw_buffer_load_b128(rpc, vpc + 16 * g4, 0, 0);
    u32x2 u1 = __builtin_amdgcn_raw_buffer_load_b64(rpc, vpc + 64 + 8 * g4, 0, 0);
    f32x4 a0 = {bflo(u0[0]), bfhi(u0[0]), bflo(u0[1]), bfhi(u0[1])};
    f32x4 a1 = {bflo(u0[2]), bfhi(u0[2]), bflo(u0[3]), bfhi(u0[3])};
    f32x4 a2 = {bflo(u1[0]), bfhi(u1[0]), bflo(u1[1]), bfhi(u1[1])};
    bf16x8 bb[3] = {__builtin_bit_cast(bf16x8, b0), __builtin_bit_cast(bf16x8, b1), __builtin_bit_cast(bf16x8, b2)};
#pragma unroll
    for (int ks = 0; ks < 3; ++ks) {
      a0 = __builtin_amdgcn_mfma_f32_16x16x32_bf16(wl[ks][0], bb[ks], a0, 0, 0, 0);
      a1 = __builtin_amdgcn_mfma_f32_16x16x32_bf16(wl[ks][1], bb[ks], a1, 0, 0, 0);
      a2 = __builtin_amdgcn_mfma_f32_16x16x32_bf16(wl[ks][2], bb[ks], a2, 0, 0, 0);
    }
    u32x4 w0 = {cvtpk(a0[0], a0[1]), cvtpk(a0[2], a0[3]), cvtpk(a1[0], a1[1]), cvtpk(a1[2], a1[3])};
    u32x2 w1 = {cvtpk(a2[0], a2[1]), cvtpk(a2[2], a2[3])};
    int sw = (cx >> 2) & 1;
    unsigned mb = ry * MROW + cx * PXB;
    if (ok) {
      *(u32x4*)(smem + mb + (((6 * c + g4) ^ sw) << 4)) = w0;
      *(u32x2*)(smem + mb + (((6 * c + 4 + (g4 >> 1)) ^ sw) << 4) + ((g4 & 1) << 3)) = w1;
    }
  }
  __syncthreads();

#pragma unroll 1
  for (int j = 0; j < 3; ++j) {
    unsigned rb[3];
#pragma unroll
    for (int dj = 0; dj < 3; ++dj) {
      int cx = 16 * j + dj + i16;
      rb[dj] = (6 * g) * MROW + cx * PXB + ((bs4 ^ ((cx >> 2) & 1)) << 4);
    }
    char* obase = (char*)out + ((size_t)(y0 + 6 * g) * WW + (x0 + 16 * j + i16)) * PXB + 96 * c;
    f32x4 acc[3][3];
#pragma unroll
    for (int r = 0; r < 8; ++r) {
#pragma unroll
      for (int dj = 0; dj < 3; ++dj)
#pragma unroll
        for (int ks = 0; ks < 3; ++ks) {
          bf16x8 b = *(const bf16x8*)(smem + rb[dj] + r * MROW + ks * 64);
#pragma unroll
          for (int di = 2; di >= 0; --di) {
            int yo = r - di;
            if (yo < 0 || yo >= 6) continue;
#pragma unroll
            for (int cb = 0; cb < 3; ++cb) {
              f32x4 cin = (di == 0 && dj == 0 && ks == 0) ? f32x4{0.f, 0.f, 0.f, 0.f} : acc[yo % 3][cb];
              acc[yo % 3][cb] = __builtin_amdgcn_mfma_f32_16x16x32_bf16(wo[di][dj][ks][cb], b, cin, 0, 0, 0);
            }
          }
        }
      if (r >= 2) {
        int yo = r - 2;
        f32x4 a0 = acc[yo % 3][0], a1 = acc[yo % 3][1], a2 = acc[yo % 3][2];
        u32x4 w0 = {cvtpk(a0[0], a0[1]), cvtpk(a0[2], a0[3]), cvtpk(a1[0], a1[1]), cvtpk(a1[2], a1[3])};
        u32x2 w1 = {cvtpk(a2[0], a2[1]), cvtpk(a2[2], a2[3])};
        char* o = obase + (size_t)yo * WW * PXB;
        *(u32x4*)(o + 16 * g4) = w0;
        *(u32x2*)(o + 64 + 8 * g4) = w1;
      }
    }
  }
}

extern "C" void kernel_launch(void* const* d_in, const int* in_sizes, int n_in,
                              void* d_out, int out_size, void* d_ws, size_t ws_size,
                              hipStream_t stream) {
  fpn_kernel<<<256, 256, 0, stream>>>((const __bf16*)d_in[0], (const __bf16*)d_in[1],
                                      (const __bf16*)d_in[2], (const __bf16*)d_in[3], (__bf16*)d_out);
}
```

```cpp
#include <hip/hip_runtime.h>
#include <hip/hip_bf16.h>

__global__ __launch_bounds__(256, 1)
void fpn_lateral_conv_kernel(const void* __restrict__ cf, const void* __restrict__ pc, const void* __restrict__ wlat,
                             const void* __restrict__ wout, void* __restrict__ out) {
  __shared__ __attribute__((aligned(16))) char smem[163840];
  unsigned tid = threadIdx.x;
  unsigned bid = blockIdx.x + (unsigned)(size_t)(__attribute__((address_space(3))) char*)smem;
  const void* ka = (const void*)__builtin_amdgcn_kernarg_segment_ptr();
  asm volatile(
    "s_load_dwordx8 s[4:11], s[0:1], 0x0\n"
    "s_load_dwordx2 s[12:13], s[0:1], 0x20\n"
    "s_getpc_b64 s[16:17]\n"
    ".Lpc0:\n"
    "v_lshlrev_b32 v1, 7, v0\n"
    "v_add_u32 v2, 0x8000, v1\n"
    "v_min_u32 v2, .LvE-.Lpc0-4, v2\n"
    "global_load_dword a219, v1, s[16:17]\n"
    "global_load_dword a219, v2, s[16:17]\n"
    "v_readfirstlane_b32 s15, v0\n"
    "s_mov_b32 s14, s2\n"
    "s_nop 3\n"
    "s_lshr_b32 s15, s15, 6\n"
    "v_and_b32 v64, 63, v0\n"
    "v_lshlrev_b32 v251, 7, v0\n"
    "v_lshlrev_b32 v31, 4, v64\n"
    "s_lshl_b32 s45, s15, 10\n"
    "s_lshl_b32 s46, s15, 9\n"
    "s_cmp_ge_u32 s15, 2\n"
    "s_cselect_b32 s87, 0xc000, 0\n"
    "s_add_u32 s87, s87, s45\n"
    "s_add_u32 s87, s87, 0x1000\n"
    "s_lshr_b32 s3, s14, 3\n"
    "s_and_b32 s2, s14, 7\n"
    "s_and_b32 s0, s15, 1\n"
    "s_lshr_b32 s1, s15, 1\n"
    "s_mul_i32 s33, s3, 41\n"
    "s_mul_i32 s34, s15, 11\n"
    "s_add_u32 s33, s33, s34\n"
    "v_min_u32 v250, 10, v64\n"
    "v_add_lshl_u32 v250, v250, s33, 7\n"
    "v_and_b32 v65, 15, v64\n"
    "v_lshrrev_b32 v66, 4, v64\n"
    "v_and_b32 v67, 7, v64\n"
    "v_bfe_u32 v68, v64, 3, 1\n"
    "v_add_u32 v69, 1, v65\n"
    "v_lshrrev_b32 v69, 1, v69\n"
    "v_bfrev_b32 v70, 1\n"
    "v_lshrrev_b32 v71, 2, v65\n"
    "v_and_b32 v72, 3, v65\n"
    "v_lshl_add_u32 v73, v66, 3, v71\n"
    "v_mul_u32_u24 v73, 0xc0, v73\n"
    "s_mul_i32 s34, s0, 96\n"
    "v_add_u32 v73, s34, v73\n"
    "v_lshl_add_u32 v36, v72, 4, v73\n"
    "v_lshl_add_u32 v37, v72, 3, v73\n"
    "v_add_u32 v37, 64, v37\n"
    "s_mul_i32 s35, s1, 0x3000\n"
    "s_sub_u32 s94, 0xc600, s35\n"
    "s_add_u32 s35, s35, 0x9600\n"
    "v_add_u32 v32, s35, v36\n"
    "v_add_u32 v33, s35, v37\n"
    "v_add_u32 v34, s94, v36\n"
    "v_add_u32 v35, s94, v37\n"
    "v_add_u32 v61, 0xae00, v36\n"
    "v_add_u32 v62, 0xae00, v37\n"
    "v_and_b32 v74, 1, v72\n"
    "v_lshlrev_b32 v74, 4, v74\n"
    "v_mov_b32 v75, 0x3f00\n"
    "v_lshlrev_b32 v75, v74, v75\n"
    "v_cmp_eq_u32 vcc, v66, v71\n"
    "s_nop 1\n"
    "v_cndmask_b32 v75, 0, v75, vcc\n"
    "v_cmp_gt_u32 vcc, 2, v72\n"
    "s_nop 1\n"
    "v_cndmask_b32 v48, 0, v75, vcc\n"
    "v_sub_u32 v49, v75, v48\n"
    "v_mov_b32 v50, v48\n"
    "v_mov_b32 v51, v49\n"
    "s_mul_i32 s90, s1, 0x80008000\n"
    "v_lshlrev_b32 v74, 2, v48\n"
    "v_and_or_b32 v52, v74, s90, v48\n"
    "v_mov_b32 v56, v52\n"
    "v_mov_b32 v54, 0\n"
    "v_lshlrev_b32 v74, 2, v49\n"
    "v_and_or_b32 v53, v74, s90, v49\n"
    "v_mov_b32 v57, v53\n"
    "v_mov_b32 v55, 0\n"
    "s_mul_i32 s48, s3, 0xd8000\n"
    "s_mul_i32 s33, s2, 0x2400\n"
    "s_add_u32 s48, s48, s33\n"
    "s_add_u32 s48, s48, 0xc0\n"
    "s_mul_i32 s49, s3, 0x36000\n"
    "s_lshr_b32 s33, s33, 1\n"
    "s_add_u32 s49, s49, s33\n"
    "s_cmp_eq_u32 s3, 0\n"
    "s_cselect_b32 s91, 0x12000, 0\n"
    "s_sub_u32 s95, s48, 0x12000\n"
    "s_add_u32 s95, s95, s91\n"
    "v_and_b32 v79, 31, v64\n"
    "s_lshl_b32 s33, s15, 6\n"
    "s_add_u32 s33, s33, 0\n"
    "v_add_u32 v80, s33, v64\n"
    "v_mul_u32_u24 v81, 0xaaab, v80\n"
    "v_lshrrev_b32 v81, 19, v81\n"
    "v_mad_i32_i24 v82, v81, -12, v80\n"
    "v_bfe_u32 v80, v81, 1, 2\n"
    "v_xor_b32 v82, v82, v80\n"
    "v_mul_u32_u24 v81, 0xc0, v81\n"
    "v_lshl_add_u32 v18, v82, 4, v81\n"
    "v_subrev_u32 v38, s91, v18\n"
    "s_lshl_b32 s33, s15, 6\n"
    "s_add_u32 s33, s33, 256\n"
    "v_add_u32 v84, s33, v64\n"
    "v_mul_u32_u24 v85, 0xaaab, v84\n"
    "v_lshrrev_b32 v85, 19, v85\n"
    "v_mad_i32_i24 v86, v85, -12, v84\n"
    "v_bfe_u32 v84, v85, 1, 2\n"
    "v_xor_b32 v86, v86, v84\n"
    "v_mul_u32_u24 v85, 0xc0, v85\n"
    "v_lshl_add_u32 v19, v86, 4, v85\n"
    "v_subrev_u32 v39, s91, v19\n"
    "s_lshl_b32 s33, s15, 6\n"
    "s_add_u32 s33, s33, 512\n"
    "v_add_u32 v80, s33, v64\n"
    "v_mul_u32_u24 v81, 0xaaab, v80\n"
    "v_lshrrev_b32 v81, 19, v81\n"
    "v_mad_i32_i24 v82, v81, -12, v80\n"
    "v_bfe_u32 v80, v81, 1, 2\n"
    "v_xor_b32 v82, v82, v80\n"
    "v_mul_u32_u24 v81, 0xc0, v81\n"
    "v_lshl_add_u32 v20, v82, 4, v81\n"
    "s_cmp_eq_u32 s2, 7\n"
    "s_cselect_b32 s34, 0x2340, -1\n"
    "s_cmp_eq_u32 s15, 0\n"
    "s_cselect_b32 s33, 0, 0xfc00\n"
    "v_cmp_eq_u32 vcc, s34, v81\n"
    "s_nop 1\n"
    "v_cndmask_b32 v20, v20, v70, vcc\n"
    "v_add_u32 v20, s33, v20\n"
    "v_subrev_u32 v40, s91, v20\n"
    "s_lshl_b32 s33, s15, 6\n"
    "s_add_u32 s33, s33, 768\n"
    "v_add_u32 v84, s33, v64\n"
    "v_mul_u32_u24 v85, 0xaaab, v84\n"
    "v_lshrrev_b32 v85, 19, v85\n"
    "v_mad_i32_i24 v86, v85, -12, v84\n"
    "v_bfe_u32 v84, v85, 1, 2\n"
    "v_xor_b32 v86, v86, v84\n"
    "v_mul_u32_u24 v85, 0xc0, v85\n"
    "v_lshl_add_u32 v21, v86, 4, v85\n"
    "v_add_u32 v21, 0xfc00, v21\n"
    "v_subrev_u32 v41, s91, v21\n"
    "s_lshl_b32 s33, s15, 5\n"
    "s_add_u32 s33, s33, 0x400\n"
    "v_add_u32 v80, s33, v79\n"
    "v_mul_u32_u24 v81, 0xaaab, v80\n"
    "v_lshrrev_b32 v81, 19, v81\n"
    "v_mad_i32_i24 v82, v81, -12, v80\n"
    "v_bfe_u32 v80, v81, 1, 2\n"
    "v_xor_b32 v82, v82, v80\n"
    "v_mul_u32_u24 v81, 0xc0, v81\n"
    "v_lshl_add_u32 v22, v82, 4, v81\n"
    "s_cmp_eq_u32 s2, 7\n"
    "s_cselect_b32 s34, 0x4740, -1\n"
    "v_cmp_eq_u32 vcc, s34, v81\n"
    "s_nop 1\n"
    "v_cndmask_b32 v22, v22, v70, vcc\n"
    "v_add_u32 v22, 0xfc00, v22\n"
    "v_subrev_u32 v42, s91, v22\n"
    "v_mul_u32_u24 v88, 0xc0, v69\n"
    "s_mul_i32 s33, s1, 0x600\n"
    "s_mul_i32 s34, s0, 96\n"
    "s_add_u32 s33, s33, s34\n"
    "v_add_u32 v88, s33, v88\n"
    "v_lshl_add_u32 v23, v66, 4, v88\n"
    "v_lshl_add_u32 v24, v66, 3, v88\n"
    "v_add_u32 v89, 1, v67\n"
    "v_lshrrev_b32 v89, 1, v89\n"
    "v_mul_u32_u24 v89, 0xc0, v89\n"
    "v_mul_u32_u24 v90, 0x9000, v68\n"
    "s_mul_i32 s33, s1, 0x300\n"
    "s_add_u32 s33, s33, s34\n"
    "s_add_u32 s33, s33, 0xc00\n"
    "s_lshl_b32 s35, s1, 3\n"
    "s_add_u32 s35, s35, s2\n"
    "s_cmp_eq_u32 s35, 15\n"
    "s_cselect_b32 s35, 7, 99\n"
    "v_add3_u32 v89, v89, v90, s33\n"
    "v_cmp_eq_u32 vcc, s35, v67\n"
    "s_nop 1\n"
    "v_cndmask_b32 v89, v89, v70, vcc\n"
    "v_lshl_add_u32 v25, v66, 4, v89\n"
    "v_lshl_add_u32 v26, v66, 3, v89\n"
    "s_sub_u32 s33, s49, 0x9000\n"
    "v_add_u32 v43, s33, v23\n"
    "v_add_u32 v44, s33, v24\n"
    "v_add_u32 v45, s33, v25\n"
    "v_add_u32 v46, s33, v26\n"
    "s_add_u32 s35, s2, s1\n"
    "s_cmp_eq_u32 s35, 0\n"
    "s_cselect_b32 s35, 0x80000000, 0\n"
    "s_mul_i32 s94, s1, 0xc0\n"
    "s_add_u32 s94, s94, s48\n"
    "s_sub_u32 s94, s94, 0x12180\n"
    "s_add_u32 s94, s94, s35\n"
    "s_mul_i32 s90, s1, 0xc0\n"
    "s_add_u32 s90, s90, s33\n"
    "s_sub_u32 s90, s90, 0xc0\n"
    "s_add_u32 s90, s90, s34\n"
    "s_add_u32 s90, s90, s35\n"
    "v_mul_u32_u24 v91, 0x12000, v65\n"
    "v_lshl_add_u32 v91, v66, 4, v91\n"
    "v_add_u32 v63, s94, v91\n"
    "v_mul_u32_u24 v92, 0x9000, v69\n"
    "v_add_u32 v92, s90, v92\n"
    "v_lshl_add_u32 v47, v66, 4, v92\n"
    "v_lshl_add_u32 v60, v66, 3, v92\n"
    "s_mul_i32 s33, s0, 6\n"
    "s_add_u32 s34, s33, 4\n"
    "s_mul_i32 s35, s1, 0x2580\n"
    "s_mul_i32 s94, s35, 3\n"
    "s_add_u32 s35, s35, 0x2580\n"
    "v_add_u32 v100, s33, v66\n"
    "v_lshrrev_b32 v101, 1, v66\n"
    "v_add_u32 v101, s34, v101\n"
    "v_and_b32 v102, 1, v66\n"
    "v_lshlrev_b32 v102, 3, v102\n"
    "v_bfe_u32 v104, v65, 1, 2\n"
    "v_mul_u32_u24 v105, 0xc0, v65\n"
    "v_xor_b32 v106, v104, v66\n"
    "v_lshl_add_u32 v106, v106, 4, v105\n"
    "v_add_u32 v0, s35, v106\n"
    "v_add_u32 v3, s94, v106\n"
    "s_mul_i32 s90, s1, 0xc00\n"
    "s_add_u32 s91, s90, 0x1bc00\n"
    "v_add_u32 v6, s91, v106\n"
    "v_add_u32 v107, 1, v65\n"
    "v_bfe_u32 v108, v107, 1, 2\n"
    "v_mul_u32_u24 v109, 0xc0, v107\n"
    "v_xor_b32 v110, v108, v66\n"
    "v_lshl_add_u32 v110, v110, 4, v109\n"
    "v_add_u32 v1, s35, v110\n"
    "v_add_u32 v4, s94, v110\n"
    "v_add_u32 v111, 2, v65\n"
    "v_bfe_u32 v112, v111, 1, 2\n"
    "v_mul_u32_u24 v113, 0xc0, v111\n"
    "v_xor_b32 v114, v112, v66\n"
    "v_lshl_add_u32 v114, v114, 4, v113\n"
    "v_add_u32 v2, s35, v114\n"
    "v_add_u32 v5, s94, v114\n"
    "v_xor_b32 v8, v100, v112\n"
    "v_lshl_add_u32 v8, v8, 4, v113\n"
    "v_add_u32 v8, s90, v8\n"
    "v_xor_b32 v9, v101, v112\n"
    "v_lshl_add_u32 v9, v9, 4, v113\n"
    "v_add3_u32 v9, v9, v102, s90\n"
    "v_add_u32 v10, 0x9600, v8\n"
    "v_add_u32 v11, 0x9600, v9\n"
    "s_add_u32 s33, s90, 0x18c00\n"
    "v_xor_b32 v27, v100, v104\n"
    "s_waitcnt lgkmcnt(0)\n"
    "s_mov_b32 s96, s10\n"
    "s_and_b32 s97, s11, 0xffff\n"
    "s_mov_b32 s98, 0x28800\n"
    "s_mov_b32 s99, 0x20000\n"
    "s_mov_b32 s28, s8\n"
    "s_and_b32 s29, s9, 0xffff\n"
    "s_mov_b32 s30, 0x4800\n"
    "s_mov_b32 s31, 0x20000\n"
    "buffer_load_dword a218, v250, s[96:99], 0 offen offset:0 \n"
    "s_add_u32 m0, s45, 0x4800\n"
    "s_nop 0\n"
    "buffer_load_dwordx4 v31, s[28:31], s45 offen  lds\n"
    "s_add_u32 m0, s45, 0x5800\n"
    "s_add_u32 s53, s45, 0x1000\n"
    "buffer_load_dwordx4 v31, s[28:31], s53 offen  lds\n"
    "s_add_u32 m0, s45, 0x6800\n"
    "s_add_u32 s54, s45, 0x2000\n"
    "buffer_load_dwordx4 v31, s[28:31], s54 offen  lds\n"
    "s_add_u32 m0, s45, 0x7800\n"
    "s_add_u32 s55, s45, 0x3000\n"
    "buffer_load_dwordx4 v31, s[28:31], s55 offen  lds\n"
    "s_add_u32 m0, s46, 0x8800\n"
    "s_add_u32 s52, s46, 0x4000\n"
    "s_mov_b32 exec_hi, 0\n"
    "buffer_load_dwordx4 v31, s[28:31], s52 offen  lds\n"
    "s_mov_b32 exec_hi, -1\n"
    "s_add_u32 m0, s45, 0x0\n"
    "s_nop 0\n"
    "buffer_load_dwordx4 v31, s[96:99], s45 offen  lds\n"
    "s_mov_b32 s16, s4\n"
    "s_and_b32 s17, s5, 0xffff\n"
    "s_mov_b32 s18, 0x1b00000\n"
    "s_mov_b32 s19, 0x20000\n"
    "s_mov_b32 s20, s6\n"
    "s_and_b32 s21, s7, 0xffff\n"
    "s_mov_b32 s22, 0x6c0000\n"
    "s_mov_b32 s23, 0x20000\n"
    "v_lshl_add_u32 v27, v27, 4, v105\n"
    "v_add_u32 v27, s33, v27\n"
    "s_add_u32 m0, s45, 0x1000\n"
    "s_nop 0\n"
    "buffer_load_dwordx4 v31, s[96:99], s87 offen  lds\n"
    "v_xor_b32 v28, v101, v104\n"
    "v_lshl_add_u32 v28, v28, 4, v105\n"
    "v_add3_u32 v28, v28, v102, s33\n"
    "v_bfe_u32 v115, v67, 1, 2\n"
    "v_mul_u32_u24 v116, 0xc0, v67\n"
    "v_xor_b32 v117, v115, v66\n"
    "v_lshl_add_u32 v117, v117, 4, v116\n"
    "v_mul_u32_u24 v121, 0x2400, v68\n"
    "s_mul_i32 s34, s1, 0x600\n"
    "s_add_u32 s35, s34, 0x1d400\n"
    "s_add_u32 s34, s34, 0x1980\n"
    "s_add_u32 m0, s45, 0x2000\n"
    "s_add_u32 s53, s45, 0xe000\n"
    "buffer_load_dwordx4 v31, s[96:99], s53 offen  lds\n"
    "v_add3_u32 v7, v117, v121, s35\n"
    "v_add_u32 v118, 2, v67\n"
    "v_bfe_u32 v119, v118, 1, 2\n"
    "v_xor_b32 v14, v100, v119\n"
    "v_lshl_add_u32 v14, v14, 4, v116\n"
    "v_add_u32 v14, s34, v14\n"
    "v_xor_b32 v15, v101, v119\n"
    "v_lshl_add_u32 v15, v15, 4, v116\n"
    "v_add3_u32 v15, v15, v102, s34\n"
    "v_mul_u32_u24 v121, 0x2580, v68\n"
    "s_add_u32 m0, s45, 0x3000\n"
    "s_add_u32 s54, s45, 0x1b000\n"
    "buffer_load_dwordx4 v31, s[96:99], s54 offen  lds\n"
    "v_mad_u32_u24 v12, v121, 3, v14\n"
    "v_mad_u32_u24 v13, v121, 3, v15\n"
    "v_add_u32 v121, 0x2580, v121\n"
    "s_add_u32 m0, s46, 0x4000\n"
    "s_add_u32 s55, s46, 0x1c000\n"
    "s_mov_b32 exec_hi, 0\n"
    "buffer_load_dwordx4 v31, s[96:99], s55 offen  lds\n"
    "s_mov_b32 exec_hi, -1\n"
    "v_add_u32 v14, v121, v14\n"
    "v_add_u32 v15, v121, v15\n"
    "s_mul_i32 s34, s15, 0xc00\n"
    "s_add_u32 s34, s34, 0x12c00\n"
    "s_xor_b32 s35, s15, 2\n"
    "s_mul_i32 s35, s35, 0xc00\n"
    "s_add_u32 s35, s35, 0x12c00\n"
    "v_add_u32 v16, s34, v31\n"
    "s_add_u32 m0, s45, 0x1bc00\n"
    "s_add_u32 s52, s45, 0x1800\n"
    "buffer_load_dwordx4 v31, s[96:99], s52 offen  lds\n"
    "v_add_u32 v17, s35, v31\n"
    "v_min_u32 v30, 0x170, v31\n"
    "s_mul_i32 s34, s15, 0x180\n"
    "s_add_u32 s34, s34, 0x24c00\n"
    "s_mul_i32 s35, s15, 0x2580\n"
    "v_add_u32 v29, s34, v30\n"
    "v_add_u32 v30, s35, v30\n"
    "s_mul_i32 s34, s0, 96\n"
    "v_add_u32 v115, s34, v64\n"
    "v_mul_u32_u24 v116, 0xaaab, v115\n"
    "v_lshrrev_b32 v116, 19, v116\n"
    "v_mad_i32_i24 v117, v116, -12, v115\n"
    "v_bfe_u32 v115, v116, 1, 2\n"
    "v_xor_b32 v117, v117, v115\n"
    "v_mul_u32_u24 v116, 0xc0, v116\n"
    "v_lshl_add_u32 v117, v117, 4, v116\n"
    "v_add_u32 v248, s33, v117\n"
    "s_mul_i32 s34, s0, 96\n"
    "s_add_u32 s34, s34, 64\n"
    "v_and_b32 v115, 31, v64\n"
    "v_add_u32 v115, s34, v115\n"
    "v_mul_u32_u24 v116, 0xaaab, v115\n"
    "v_lshrrev_b32 v116, 19, v116\n"
    "v_mad_i32_i24 v117, v116, -12, v115\n"
    "v_bfe_u32 v115, v116, 1, 2\n"
    "v_xor_b32 v117, v117, v115\n"
    "v_mul_u32_u24 v116, 0xc0, v116\n"
    "v_lshl_add_u32 v117, v117, 4, v116\n"
    "v_add_u32 v249, s33, v117\n"
    "v_and_b32 v103, 1, v65\n"
    "v_lshrrev_b32 v104, 1, v65\n"
    "v_lshlrev_b32 v107, 4, v100\n"
    "v_lshl_add_u32 v108, v101, 4, v102\n"
    "s_mul_i32 s34, s1, 0xc0\n"
    "v_add_u32 v107, s34, v107\n"
    "v_add_u32 v108, s34, v108\n"
    "v_mul_u32_u24 v106, 0x600, v104\n"
    "v_mul_u32_u24 v105, 0x480, v103\n"
    "v_add_u32 v106, v106, v105\n"
    "v_add_u32 v106, 0x24600, v106\n"
    "v_mul_u32_u24 v105, 0x7080, v103\n"
    "v_cmp_gt_u32 vcc, 2, v65\n"
    "s_nop 1\n"
    "v_cndmask_b32 v106, v106, v105, vcc\n"
    "v_add_u32 v244, v106, v107\n"
    "v_add_u32 v245, v106, v108\n"
    "v_cmp_eq_u32 vcc, 0, v69\n"
    "s_nop 1\n"
    "v_cndmask_b32_e64 v111, v69, 7, vcc\n"
    "v_mul_u32_u24 v110, 0x600, v111\n"
    "v_mul_u32_u24 v109, 0x180, v103\n"
    "v_sub_u32 v110, v110, v109\n"
    "v_add_u32 v110, 0x24300, v110\n"
    "v_mul_u32_u24 v109, 0x2580, v103\n"
    "v_sub_u32 v109, 0x4b00, v109\n"
    "v_cmp_eq_u32 vcc, 1, v69\n"
    "s_nop 1\n"
    "v_cndmask_b32 v110, v110, v109, vcc\n"
    "v_add_u32 v246, v110, v107\n"
    "v_add_u32 v247, v110, v108\n"
    "s_lshl_b32 s44, s1, 31\n"
    "s_or_b32 s44, s44, 0x3f800000\n"
    "s_mov_b32 s50, 0xaaaaaaaa\n"
    "s_mov_b32 s51, 0xaaaaaaaa\n"
    "s_mul_i32 s47, s1, 0x12000\n"
    "s_add_u32 s47, s47, s48\n"
    "s_mul_i32 s34, s0, 0x600\n"
    "s_add_u32 s47, s47, s34\n"
    "s_sub_u32 s47, s47, 0xc0\n"
    "s_mov_b32 s24, s12\n"
    "s_and_b32 s25, s13, 0xffff\n"
    "s_mov_b32 s26, 0x1b00000\n"
    "s_mov_b32 s27, 0x20000\n"
    "s_add_u32 s34, s48, 0xc6000\n"
    "s_add_u32 s36, s4, s34\n"
    "s_addc_u32 s37, s5, 0\n"
    "s_and_b32 s37, s37, 0xffff\n"
    "s_sub_u32 s38, 0x1b00000, s34\n"
    "s_mov_b32 s39, 0x20000\n"
    "s_add_u32 s34, s49, 0x2d000\n"
    "s_add_u32 s40, s6, s34\n"
    "s_addc_u32 s41, s7, 0\n"
    "s_and_b32 s41, s41, 0xffff\n"
    "s_sub_u32 s42, 0x6c0000, s34\n"
    "s_mov_b32 s43, 0x20000\n"
    "s_add_u32 s34, s49, 0x36000\n"
    "s_min_u32 s34, s34, 0x6c0000\n"
    "s_add_u32 s56, s6, s34\n"
    "s_addc_u32 s57, s7, 0\n"
    "s_and_b32 s57, s57, 0xffff\n"
    "s_sub_u32 s58, 0x6c0000, s34\n"
    "s_mov_b32 s59, 0x20000\n"
    "v_add_u32 v32, 0xffff6a00, v32\n"
    "v_add_u32 v33, 0xffff6a00, v33\n"
    "v_add_u32 v34, 0xffff6a00, v34\n"
    "v_add_u32 v35, 0xffff6a00, v35\n"
    "v_add_u32 v61, 0xffff6a00, v61\n"
    "v_add_u32 v62, 0xffff6a00, v62\n"
    "s_add_u32 m0, s45, 0x1cc00\n"
    "s_add_u32 s53, s87, 0x1800\n"
    "buffer_load_dwordx4 v31, s[96:99], s53 offen  lds\n"
    "s_add_u32 m0, s45, 0x1dc00\n"
    "s_add_u32 s54, s45, 0xf800\n"
    "buffer_load_dwordx4 v31, s[96:99], s54 offen  lds\n"
    "s_add_u32 m0, s45, 0x1ec00\n"
    "s_add_u32 s55, s45, 0x1c800\n"
    "buffer_load_dwordx4 v31, s[96:99], s55 offen  lds\n"
    "s_add_u32 m0, s46, 0x1fc00\n"
    "s_add_u32 s52, s46, 0x1d800\n"
    "s_mov_b32 exec_hi, 0\n"
    "buffer_load_dwordx4 v31, s[96:99], s52 offen  lds\n"
    "s_mov_b32 exec_hi, -1\n"
    "s_add_u32 m0, s45, 0x20400\n"
    "s_add_u32 s53, s45, 0x3000\n"
    "buffer_load_dwordx4 v31, s[96:99], s53 offen  lds\n"
    "s_add_u32 m0, s45, 0x21400\n"
    "s_add_u32 s54, s87, 0x3000\n"
    "buffer_load_dwordx4 v31, s[96:99], s54 offen  lds\n"
    "s_add_u32 m0, s45, 0x22400\n"
    "s_add_u32 s55, s45, 0x11000\n"
    "buffer_load_dwordx4 v31, s[96:99], s55 offen  lds\n"
    "s_add_u32 m0, s45, 0x23400\n"
    "s_add_u32 s52, s45, 0x1e000\n"
    "buffer_load_dwordx4 v31, s[96:99], s52 offen  lds\n"
    "s_add_u32 m0, s46, 0x24400\n"
    "s_add_u32 s53, s46, 0x1f000\n"
    "s_mov_b32 exec_hi, 0\n"
    "buffer_load_dwordx4 v31, s[96:99], s53 offen  lds\n"
    "s_mov_b32 exec_hi, -1\n"
    "s_add_u32 m0, s45, 0x9600\n"
    "s_add_u32 s54, s45, 0x4800\n"
    "buffer_load_dwordx4 v31, s[96:99], s54 offen  lds\n"
    "s_waitcnt vmcnt(11)\n"
    "s_barrier\n"
    "ds_read_b64_tr_b16 a[220:221], v36 offset:18432\n"
    "s_add_u32 m0, s45, 0xa600\n"
    "s_add_u32 s55, s87, 0x4800\n"
    "buffer_load_dwordx4 v31, s[96:99], s55 offen  lds\n"
    "ds_read_b64_tr_b16 a[222:223], v36 offset:19200\n"
    "ds_read_b64_tr_b16 a[224:225], v36 offset:18440\n"
    "ds_read_b64_tr_b16 a[226:227], v36 offset:19208\n"
    "s_add_u32 m0, s45, 0xb600\n"
    "s_add_u32 s52, s45, 0x12800\n"
    "buffer_load_dwordx4 v31, s[96:99], s52 offen  lds\n"
    "ds_read_b64_tr_b16 a[228:229], v37 offset:18432\n"
    "ds_read_b64_tr_b16 a[230:231], v37 offset:19200\n"
    "ds_read_b64_tr_b16 a[232:233], v36 offset:24576\n"
    "ds_read_b64_tr_b16 a[234:235], v36 offset:25344\n"
    "s_add_u32 m0, s45, 0xc600\n"
    "s_add_u32 s53, s45, 0x1f800\n"
    "buffer_load_dwordx4 v31, s[96:99], s53 offen  lds\n"
    "ds_read_b64_tr_b16 a[236:237], v36 offset:24584\n"
    "ds_read_b64_tr_b16 a[238:239], v36 offset:25352\n"
    "ds_read_b64_tr_b16 a[240:241], v37 offset:24576\n"
    "ds_read_b64_tr_b16 a[242:243], v37 offset:25344\n"
    "s_add_u32 m0, s46, 0xd600\n"
    "s_add_u32 s54, s46, 0x20800\n"
    "s_mov_b32 exec_hi, 0\n"
    "buffer_load_dwordx4 v31, s[96:99], s54 offen  lds\n"
    "s_mov_b32 exec_hi, -1\n"
    "ds_read_b64_tr_b16 a[244:245], v36 offset:30720\n"
    "ds_read_b64_tr_b16 a[246:247], v36 offset:31488\n"
    "ds_read_b64_tr_b16 a[248:249], v36 offset:30728\n"
    "s_add_u32 m0, s45, 0xde00\n"
    "s_add_u32 s55, s45, 0x6000\n"
    "buffer_load_dwordx4 v31, s[96:99], s55 offen  lds\n"
    "ds_read_b64_tr_b16 a[250:251], v36 offset:31496\n"
    "ds_read_b64_tr_b16 a[252:253], v37 offset:30720\n"
    "ds_read_b64_tr_b16 a[254:255], v37 offset:31488\n"
    "s_add_u32 m0, s45, 0xee00\n"
    "s_add_u32 s52, s87, 0x6000\n"
    "buffer_load_dwordx4 v31, s[96:99], s52 offen  lds\n"
    "ds_read_b64_tr_b16 a[0:1], v34 offset:0\n"
    "ds_read_b64_tr_b16 a[2:3], v32 offset:0\n"
    "ds_read_b64_tr_b16 a[4:5], v32 offset:768\n"
    "s_add_u32 m0, s45, 0xfe00\n"
    "s_add_u32 s53, s45, 0x14000\n"
    "buffer_load_dwordx4 v31, s[96:99], s53 offen  lds\n"
    "ds_read_b64_tr_b16 a[6:7], v34 offset:768\n"
    "ds_read_b64_tr_b16 v[64:65], v61 offset:0\n"
    "ds_read_b64_tr_b16 v[66:67], v61 offset:768\n"
    "ds_read_b64_tr_b16 a[8:9], v34 offset:8\n"
    "s_add_u32 m0, s45, 0x10e00\n"
    "s_add_u32 s54, s45, 0x21000\n"
    "buffer_load_dwordx4 v31, s[96:99], s54 offen  lds\n"
    "ds_read_b64_tr_b16 a[10:11], v32 offset:8\n"
    "s_waitcnt vmcnt(14) lgkmcnt(8)\n"
    "s_barrier\n"
    "s_waitcnt lgkmcnt(6)\n"
    "ds_read_b64_tr_b16 a[12:13], v32 offset:776\n"
    "v_mfma_f32_16x16x32_bf16 v[76:79], v[48:51], a[0:3], 0\n"
    "ds_read_b64_tr_b16 a[14:15], v34 offset:776\n"
    "v_add_u32 v32, 0x1bc00, v32\n"
    "v_add_u32 v34, 0x1bc00, v34\n"
    "s_add_u32 m0, s46, 0x11e00\n"
    "s_add_u32 s55, s46, 0x22000\n"
    "s_mov_b32 exec_hi, 0\n"
    "buffer_load_dwordx4 v31, s[96:99], s55 offen  lds\n"
    "s_mov_b32 exec_hi, -1\n"
    "s_waitcnt lgkmcnt(6)\n"
    "v_mfma_f32_16x16x32_bf16 v[80:83], v[48:51], a[4:7], 0\n"
    "ds_read_b64_tr_b16 v[68:69], v61 offset:8\n"
    "s_waitcnt lgkmcnt(5)\n"
    "v_mfma_f32_16x16x32_bf16 v[76:79], v[52:55], v[64:67], v[76:79]\n"
    "ds_read_b64_tr_b16 v[70:71], v61 offset:776\n"
    "v_add_u32 v61, 0x1bc00, v61\n"
    "v_mfma_f32_16x16x32_bf16 v[80:83], v[54:57], v[64:67], v[80:83]\n"
    "ds_read_b64_tr_b16 a[16:17], v35 offset:0\n"
    "s_waitcnt lgkmcnt(5)\n"
    "s_add_u32 m0, s45, 0x12600\n"
    "s_add_u32 s52, s45, 0x7800\n"
    "buffer_load_dwordx4 v31, s[96:99], s52 offen  lds\n"
    "v_mfma_f32_16x16x32_bf16 v[84:87], v[48:51], a[8:11], 0\n"
    "ds_read_b64_tr_b16 a[18:19], v33 offset:0\n"
    "v_cvt_pk_bf16_f32 v92, v76, v77\n"
    "s_waitcnt lgkmcnt(4)\n"
    "v_cvt_pk_bf16_f32 v93, v78, v79\n"
    "v_cvt_pk_bf16_f32 v94, v80, v81\n"
    "v_mfma_f32_16x16x32_bf16 v[88:91], v[48:51], a[12:15], 0\n"
    "ds_read_b64_tr_b16 a[20:21], v33 offset:768\n"
    "s_add_u32 m0, s45, 0x13600\n"
    "s_add_u32 s53, s87, 0x7800\n"
    "buffer_load_dwordx4 v31, s[96:99], s53 offen  lds\n"
    "v_cvt_pk_bf16_f32 v95, v82, v83\n"
    "v_accvgpr_write_b32 a6, v92\n"
    "v_accvgpr_write_b32 a7, v93\n"
    "v_accvgpr_write_b32 a8, v94\n"
    "v_accvgpr_write_b32 a9, v95\n"
    "ds_read_b64_tr_b16 a[22:23], v35 offset:768\n"
    "s_waitcnt lgkmcnt(4)\n"
    "v_add_u32 v33, 0x1bc00, v33\n"
    "ds_read_b64_tr_b16 v[72:73], v62 offset:0\n"
    "v_mfma_f32_16x16x32_bf16 v[84:87], v[52:55], v[68:71], v[84:87]\n"
    "s_add_u32 m0, s45, 0x14600\n"
    "s_add_u32 s54, s45, 0x15800\n"
    "buffer_load_dwordx4 v31, s[96:99], s54 offen  lds\n"
    "v_mfma_f32_16x16x32_bf16 v[88:91], v[54:57], v[68:71], v[88:91]\n"
    "ds_read_b64_tr_b16 v[74:75], v62 offset:768\n"
    "s_waitcnt lgkmcnt(4)\n"
    "v_add_u32 v35, 0x1bc00, v35\n"
    "v_add_u32 v62, 0x1bc00, v62\n"
    "v_mfma_f32_16x16x32_bf16 v[76:79], v[48:51], a[16:19], 0\n"
    "ds_read_b64_tr_b16 a[24:25], v34 offset:0\n"
    "v_cvt_pk_bf16_f32 v96, v84, v85\n"
    "s_add_u32 m0, s45, 0x15600\n"
    "s_add_u32 s55, s45, 0x22800\n"
    "buffer_load_dwordx4 v31, s[96:99], s55 offen  lds\n"
    "v_cvt_pk_bf16_f32 v97, v86, v87\n"
    "v_cvt_pk_bf16_f32 v98, v88, v89\n"
    "v_cvt_pk_bf16_f32 v99, v90, v91\n"
    "v_accvgpr_write_b32 a14, v96\n"
    "v_accvgpr_write_b32 a15, v97\n"
    "v_accvgpr_write_b32 a16, v98\n"
    "v_accvgpr_write_b32 a17, v99\n"
    "s_waitcnt lgkmcnt(3)\n"
    "ds_read_b64_tr_b16 a[26:27], v32 offset:0\n"
    "v_mfma_f32_16x16x32_bf16 v[80:83], v[48:51], a[20:23], 0\n"
    "ds_read_b64_tr_b16 a[28:29], v32 offset:768\n"
    "s_add_u32 m0, s46, 0x16600\n"
    "s_add_u32 s52, s46, 0x23800\n"
    "s_mov_b32 exec_hi, 0\n"
    "buffer_load_dwordx4 v31, s[96:99], s52 offen  lds\n"
    "s_mov_b32 exec_hi, -1\n"
    "s_waitcnt lgkmcnt(3)\n"
    "ds_read_b64_tr_b16 a[30:31], v34 offset:768\n"
    "v_mfma_f32_16x16x32_bf16 v[76:79], v[52:55], v[72:75], v[76:79]\n"
    "s_waitcnt vmcnt(15)\n"
    "s_barrier\n"
    "v_mfma_f32_16x16x32_bf16 v[80:83], v[54:57], v[72:75], v[80:83]\n"
    "ds_read_b64_tr_b16 v[64:65], v61 offset:0\n"
    "s_add_u32 m0, s45, 0x16e00\n"
    "s_add_u32 s53, s45, 0x9000\n"
    "buffer_load_dwordx4 v31, s[96:99], s53 offen  lds\n"
    "s_nop 0\n"
    "v_cvt_pk_bf16_f32 v92, v76, v77\n"
    "v_cvt_pk_bf16_f32 v93, v78, v79\n"
    "ds_read_b64_tr_b16 v[66:67], v61 offset:768\n"
    "v_cvt_pk_bf16_f32 v94, v80, v81\n"
    "s_waitcnt lgkmcnt(4)\n"
    "v_cvt_pk_bf16_f32 v95, v82, v83\n"
    "v_accvgpr_write_b32 a22, v92\n"
    "v_mfma_f32_16x16x32_bf16 v[84:87], v[48:51], a[24:27], 0\n"
    "ds_read_b64_tr_b16 a[32:33], v34 offset:8\n"
    "s_add_u32 m0, s45, 0x17e00\n"
    "s_add_u32 s54, s87, 0x9000\n"
    "buffer_load_dwordx4 v31, s[96:99], s54 offen  lds\n"
    "v_accvgpr_write_b32 a23, v93\n"
    "v_accvgpr_write_b32 a24, v94\n"
    "v_accvgpr_write_b32 a25, v95\n"
    "s_waitcnt lgkmcnt(3)\n"
    "ds_read_b64_tr_b16 a[34:35], v32 offset:8\n"
    "v_mfma_f32_16x16x32_bf16 v[88:91], v[48:51], a[28:31], 0\n"
    "ds_read_b64_tr_b16 a[36:37], v32 offset:776\n"
    "s_add_u32 m0, s45, 0x18e00\n"
    "s_add_u32 s55, s45, 0x17000\n"
    "buffer_load_dwordx4 v31, s[96:99], s55 offen  lds\n"
    "ds_read_b64_tr_b16 a[38:39], v34 offset:776\n"
    "ds_read_b64_tr_b16 v[68:69], v61 offset:8\n"
    "s_waitcnt lgkmcnt(5)\n"
    "v_mfma_f32_16x16x32_bf16 v[84:87], v[52:55], v[64:67], v[84:87]\n"
    "ds_read_b64_tr_b16 v[70:71], v61 offset:776\n"
    "v_mfma_f32_16x16x32_bf16 v[88:91], v[54:57], v[64:67], v[88:91]\n"
    "s_add_u32 m0, s45, 0x19e00\n"
    "s_add_u32 s52, s45, 0x24000\n"
    "buffer_load_dwordx4 v31, s[96:99], s52 offen  lds\n"
    "ds_read_b64_tr_b16 a[40:41], v35 offset:0\n"
    "s_nop 1\n"
    "v_cvt_pk_bf16_f32 v96, v84, v85\n"
    "v_cvt_pk_bf16_f32 v97, v86, v87\n"
    "s_waitcnt lgkmcnt(5)\n"
    "v_accvgpr_write_b32 a30, v96\n"
    "v_cvt_pk_bf16_f32 v98, v88, v89\n"
    "v_mfma_f32_16x16x32_bf16 v[76:79], v[48:51], a[32:35], 0\n"
    "ds_read_b64_tr_b16 a[42:43], v33 offset:0\n"
    "v_cvt_pk_bf16_f32 v99, v90, v91\n"
    "v_accvgpr_write_b32 a31, v97\n"
    "s_add_u32 m0, s46, 0x1ae00\n"
    "s_add_u32 s53, s46, 0x25000\n"
    "s_mov_b32 exec_hi, 0\n"
    "buffer_load_dwordx4 v31, s[96:99], s53 offen  lds\n"
    "s_mov_b32 exec_hi, -1\n"
    "s_waitcnt lgkmcnt(4)\n"
    "v_mfma_f32_16x16x32_bf16 v[80:83], v[48:51], a[36:39], 0\n"
    "ds_read_b64_tr_b16 a[44:45], v33 offset:768\n"
    "v_accvgpr_write_b32 a32, v98\n"
    "s_waitcnt lgkmcnt(3)\n"
    "v_accvgpr_write_b32 a33, v99\n"
    "ds_read_b64_tr_b16 a[46:47], v35 offset:768\n"
    "v_mfma_f32_16x16x32_bf16 v[76:79], v[52:55], v[68:71], v[76:79]\n"
    "ds_read_b64_tr_b16 v[72:73], v62 offset:0\n"
    "v_mfma_f32_16x16x32_bf16 v[80:83], v[54:57], v[68:71], v[80:83]\n"
    "s_add_u32 m0, s45, 0x4800\n"
    "s_add_u32 s54, s45, 0xa800\n"
    "buffer_load_dwordx4 v31, s[96:99], s54 offen  lds\n"
    "ds_read_b64_tr_b16 v[74:75], v62 offset:768\n"
    "s_nop 1\n"
    "v_cvt_pk_bf16_f32 v92, v76, v77\n"
    "s_waitcnt lgkmcnt(4)\n"
    "v_cvt_pk_bf16_f32 v93, v78, v79\n"
    "v_accvgpr_write_b32 a38, v92\n"
    "v_mfma_f32_16x16x32_bf16 v[84:87], v[48:51], a[40:43], 0\n"
    "ds_read_b64_tr_b16 a[48:49], v34 offset:18432\n"
    "s_add_u32 m0, s45, 0x5800\n"
    "s_add_u32 s55, s87, 0xa800\n"
    "buffer_load_dwordx4 v31, s[96:99], s55 offen  lds\n"
    "v_cvt_pk_bf16_f32 v94, v80, v81\n"
    "v_cvt_pk_bf16_f32 v95, v82, v83\n"
    "v_accvgpr_write_b32 a39, v93\n"
    "v_accvgpr_write_b32 a40, v94\n"
    "v_accvgpr_write_b32 a41, v95\n"
    "ds_read_b64_tr_b16 a[50:51], v32 offset:18432\n"
    "s_waitcnt lgkmcnt(4)\n"
    "ds_read_b64_tr_b16 a[52:53], v32 offset:19200\n"
    "v_mfma_f32_16x16x32_bf16 v[88:91], v[48:51], a[44:47], 0\n"
    "s_add_u32 m0, s45, 0x6800\n"
    "s_add_u32 s52, s45, 0x18800\n"
    "buffer_load_dwordx4 v31, s[96:99], s52 offen  lds\n"
    "ds_read_b64_tr_b16 a[54:55], v34 offset:19200\n"
    "s_waitcnt lgkmcnt(4)\n"
    "ds_read_b64_tr_b16 v[64:65], v61 offset:18432\n"
    "v_mfma_f32_16x16x32_bf16 v[84:87], v[52:55], v[72:75], v[84:87]\n"
    "s_waitcnt vmcnt(18)\n"
    "s_barrier\n"
    "v_mfma_f32_16x16x32_bf16 v[88:91], v[54:57], v[72:75], v[88:91]\n"
    "s_add_u32 m0, s45, 0x7800\n"
    "s_add_u32 s53, s45, 0x25800\n"
    "buffer_load_dwordx4 v31, s[96:99], s53 offen  lds\n"
    "ds_read_b64_tr_b16 v[66:67], v61 offset:19200\n"
    "s_nop 0\n"
    "v_cvt_pk_bf16_f32 v96, v84, v85\n"
    "v_cvt_pk_bf16_f32 v97, v86, v87\n"
    "v_accvgpr_write_b32 a46, v96\n"
    "s_waitcnt lgkmcnt(4)\n"
    "v_cvt_pk_bf16_f32 v98, v88, v89\n"
    "v_cvt_pk_bf16_f32 v99, v90, v91\n"
    "v_mfma_f32_16x16x32_bf16 v[76:79], v[48:51], a[48:51], 0\n"
    "ds_read_b64_tr_b16 a[56:57], v34 offset:18440\n"
    "s_add_u32 m0, s46, 0x8800\n"
    "s_add_u32 s54, s46, 0x26800\n"
    "s_mov_b32 exec_hi, 0\n"
    "buffer_load_dwordx4 v31, s[96:99], s54 offen  lds\n"
    "s_mov_b32 exec_hi, -1\n"
    "s_waitcnt lgkmcnt(3)\n"
    "v_accvgpr_write_b32 a47, v97\n"
    "v_mfma_f32_16x16x32_bf16 v[80:83], v[48:51], a[52:55], 0\n"
    "ds_read_b64_tr_b16 a[58:59], v32 offset:18440\n"
    "v_accvgpr_write_b32 a48, v98\n"
    "v_accvgpr_write_b32 a49, v99\n"
    "ds_read_b64_tr_b16 a[60:61], v32 offset:19208\n"
    "v_add_u32 v32, 0xfffeda00, v32\n"
    "ds_read_b64_tr_b16 a[62:63], v34 offset:19208\n"
    "s_add_u32 m0, s45, 0x0\n"
    "s_add_u32 s55, s45, 0xc000\n"
    "buffer_load_dwordx4 v31, s[96:99], s55 offen  lds\n"
    "s_waitcnt lgkmcnt(4)\n"
    "v_mfma_f32_16x16x32_bf16 v[76:79], v[52:55], v[64:67], v[76:79]\n"
    "ds_read_b64_tr_b16 v[68:69], v61 offset:18440\n"
    "v_add_u32 v34, 0xfffeda00, v34\n"
    "v_mfma_f32_16x16x32_bf16 v[80:83], v[54:57], v[64:67], v[80:83]\n"
    "ds_read_b64_tr_b16 v[70:71], v61 offset:19208\n"
    "v_add_u32 v61, 0xfffeda00, v61\n"
    "s_add_u32 m0, s45, 0x1000\n"
    "s_add_u32 s52, s87, 0xc000\n"
    "buffer_load_dwordx4 v31, s[96:99], s52 offen  lds\n"
    "v_cvt_pk_bf16_f32 v92, v76, v77\n"
    "v_cvt_pk_bf16_f32 v93, v78, v79\n"
    "v_accvgpr_write_b32 a54, v92\n"
    "v_cvt_pk_bf16_f32 v94, v80, v81\n"
    "s_waitcnt lgkmcnt(4)\n"
    "v_cvt_pk_bf16_f32 v95, v82, v83\n"
    "v_accvgpr_write_b32 a55, v93\n"
    "v_mfma_f32_16x16x32_bf16 v[84:87], v[48:51], a[56:59], 0\n"
    "ds_read_b64_tr_b16 a[64:65], v35 offset:18432\n"
    "s_add_u32 m0, s45, 0x2000\n"
    "s_add_u32 s53, s45, 0x1a000\n"
    "buffer_load_dwordx4 v31, s[96:99], s53 offen  lds\n"
    "s_waitcnt lgkmcnt(3)\n"
    "v_mfma_f32_16x16x32_bf16 v[88:91], v[48:51], a[60:63], 0\n"
    "ds_read_b64_tr_b16 a[66:67], v33 offset:18432\n"
    "v_accvgpr_write_b32 a56, v94\n"
    "v_accvgpr_write_b32 a57, v95\n"
    "ds_read_b64_tr_b16 a[68:69], v33 offset:19200\n"
    "v_add_u32 v33, 0xfffeda00, v33\n"
    "s_waitcnt lgkmcnt(3)\n"
    "ds_read_b64_tr_b16 a[70:71], v35 offset:19200\n"
    "v_mfma_f32_16x16x32_bf16 v[84:87], v[52:55], v[68:71], v[84:87]\n"
    "s_add_u32 m0, s45, 0x3000\n"
    "s_add_u32 s54, s45, 0x27000\n"
    "buffer_load_dwordx4 v31, s[96:99], s54 offen  lds\n"
    "v_mfma_f32_16x16x32_bf16 v[88:91], v[54:57], v[68:71], v[88:91]\n"
    "ds_read_b64_tr_b16 v[72:73], v62 offset:18432\n"
    "v_add_u32 v35, 0xfffeda00, v35\n"
    "ds_read_b64_tr_b16 v[74:75], v62 offset:19200\n"
    "s_nop 0\n"
    "v_cvt_pk_bf16_f32 v96, v84, v85\n"
    "v_cvt_pk_bf16_f32 v97, v86, v87\n"
    "v_accvgpr_write_b32 a62, v96\n"
    "v_accvgpr_write_b32 a63, v97\n"
    "v_add_u32 v62, 0xfffeda00, v62\n"
    "s_add_u32 m0, s46, 0x4000\n"
    "s_add_u32 s55, s46, 0x28000\n"
    "s_mov_b32 exec_hi, 0\n"
    "buffer_load_dwordx4 v31, s[96:99], s55 offen  lds\n"
    "s_mov_b32 exec_hi, -1\n"
    "s_waitcnt lgkmcnt(4)\n"
    "v_mfma_f32_16x16x32_bf16 v[76:79], v[48:51], a[64:67], 0\n"
    "ds_read_b64_tr_b16 a[72:73], v34 offset:0\n"
    "v_cvt_pk_bf16_f32 v98, v88, v89\n"
    "v_cvt_pk_bf16_f32 v99, v90, v91\n"
    "v_accvgpr_write_b32 a64, v98\n"
    "s_waitcnt lgkmcnt(3)\n"
    "v_accvgpr_write_b32 a65, v99\n"
    "ds_read_b64_tr_b16 a[74:75], v32 offset:0\n"
    "v_mfma_f32_16x16x32_bf16 v[80:83], v[48:51], a[68:71], 0\n"
    "s_add_u32 m0, s45, 0x1bc00\n"
    "s_nop 0\n"
    "buffer_load_dwordx4 v38, s[16:19], s95 offen nt lds\n"
    "ds_read_b64_tr_b16 a[76:77], v32 offset:768\n"
    "s_waitcnt lgkmcnt(3)\n"
    "ds_read_b64_tr_b16 a[78:79], v34 offset:768\n"
    "v_mfma_f32_16x16x32_bf16 v[76:79], v[52:55], v[72:75], v[76:79]\n"
    "s_waitcnt vmcnt(21)\n"
    "s_barrier\n"
    "v_mfma_f32_16x16x32_bf16 v[80:83], v[54:57], v[72:75], v[80:83]\n"
    "s_add_u32 m0, s45, 0x1cc00\n"
    "s_nop 0\n"
    "buffer_load_dwordx4 v39, s[16:19], s95 offen nt lds\n"
    "ds_read_b64_tr_b16 v[64:65], v61 offset:0\n"
    "s_nop 0\n"
    "v_cvt_pk_bf16_f32 v92, v76, v77\n"
    "v_cvt_pk_bf16_f32 v93, v78, v79\n"
    "ds_read_b64_tr_b16 v[66:67], v61 offset:768\n"
    "v_cvt_pk_bf16_f32 v94, v80, v81\n"
    "v_cvt_pk_bf16_f32 v95, v82, v83\n"
    "s_waitcnt lgkmcnt(4)\n"
    "s_add_u32 m0, s45, 0x1dc00\n"
    "s_nop 0\n"
    "buffer_load_dwordx4 v40, s[16:19], s95 offen nt lds\n"
    "v_mfma_f32_16x16x32_bf16 v[84:87], v[48:51], a[72:75], 0\n"
    "ds_read_b64_tr_b16 a[80:81], v34 offset:8\n"
    "v_accvgpr_write_b32 a70, v92\n"
    "v_accvgpr_write_b32 a71, v93\n"
    "v_accvgpr_write_b32 a72, v94\n"
    "v_accvgpr_write_b32 a73, v95\n"
    "s_waitcnt lgkmcnt(3)\n"
    "ds_read_b64_tr_b16 a[82:83], v32 offset:8\n"
    "v_mfma_f32_16x16x32_bf16 v[88:91], v[48:51], a[76:79], 0\n"
    "s_add_u32 m0, s45, 0x1ec00\n"
    "s_nop 0\n"
    "buffer_load_dwordx4 v41, s[16:19], s95 offen nt lds\n"
    "ds_read_b64_tr_b16 a[84:85], v32 offset:776\n"
    "ds_read_b64_tr_b16 a[86:87], v34 offset:776\n"
    "s_waitcnt lgkmcnt(4)\n"
    "ds_read_b64_tr_b16 v[68:69], v61 offset:8\n"
    "v_mfma_f32_16x16x32_bf16 v[84:87], v[52:55], v[64:67], v[84:87]\n"
    "ds_read_b64_tr_b16 v[70:71], v61 offset:776\n"
    "v_mfma_f32_16x16x32_bf16 v[88:91], v[54:57], v[64:67], v[88:91]\n"
    "s_add_u32 m0, s46, 0x1fc00\n"
    "s_mov_b32 exec_hi, 0\n"
    "buffer_load_dwordx4 v42, s[16:19], s95 offen nt lds\n"
    "s_mov_b32 exec_hi, -1\n"
    "ds_read_b64_tr_b16 a[88:89], v35 offset:0\n"
    "s_nop 0\n"
    "v_cvt_pk_bf16_f32 v96, v84, v85\n"
    "v_cvt_pk_bf16_f32 v97, v86, v87\n"
    "s_waitcnt lgkmcnt(5)\n"
    "v_accvgpr_write_b32 a78, v96\n"
    "v_cvt_pk_bf16_f32 v98, v88, v89\n"
    "v_mfma_f32_16x16x32_bf16 v[76:79], v[48:51], a[80:83], 0\n"
    "ds_read_b64_tr_b16 a[90:91], v33 offset:0\n"
    "v_cvt_pk_bf16_f32 v99, v90, v91\n"
    "buffer_load_dwordx4 v[196:199], v43, s[20:23], 0 offen offset:0 \n"
    "v_accvgpr_write_b32 a79, v97\n"
    "v_accvgpr_write_b32 a80, v98\n"
    "s_waitcnt lgkmcnt(4)\n"
    "v_accvgpr_write_b32 a81, v99\n"
    "ds_read_b64_tr_b16 a[92:93], v33 offset:768\n"
    "v_mfma_f32_16x16x32_bf16 v[80:83], v[48:51], a[84:87], 0\n"
    "ds_read_b64_tr_b16 a[94:95], v35 offset:768\n"
    "s_waitcnt lgkmcnt(4)\n"
    "buffer_load_dwordx2 v[200:201], v44, s[20:23], 0 offen offset:64 \n"
    "v_mfma_f32_16x16x32_bf16 v[76:79], v[52:55], v[68:71], v[76:79]\n"
    "ds_read_b64_tr_b16 v[72:73], v62 offset:0\n"
    "v_mfma_f32_16x16x32_bf16 v[80:83], v[54:57], v[68:71], v[80:83]\n"
    "ds_read_b64_tr_b16 v[74:75], v62 offset:768\n"
    "ds_read_b64_tr_b16 a[96:97], v34 offset:18432\n"
    "s_waitcnt lgkmcnt(5)\n"
    "buffer_load_dwordx4 v[202:205], v23, s[20:23], s49 offen offset:0 \n"
    "v_mfma_f32_16x16x32_bf16 v[84:87], v[48:51], a[88:91], 0\n"
    "ds_read_b64_tr_b16 a[98:99], v32 offset:18432\n"
    "v_cvt_pk_bf16_f32 v92, v76, v77\n"
    "v_cvt_pk_bf16_f32 v93, v78, v79\n"
    "v_cvt_pk_bf16_f32 v94, v80, v81\n"
    "v_cvt_pk_bf16_f32 v95, v82, v83\n"
    "v_accvgpr_write_b32 a86, v92\n"
    "v_accvgpr_write_b32 a87, v93\n"
    "v_accvgpr_write_b32 a88, v94\n"
    "s_waitcnt lgkmcnt(4)\n"
    "v_accvgpr_write_b32 a89, v95\n"
    "ds_read_b64_tr_b16 a[100:101], v32 offset:19200\n"
    "v_mfma_f32_16x16x32_bf16 v[88:91], v[48:51], a[92:95], 0\n"
    "buffer_load_dwordx2 v[206:207], v24, s[20:23], s49 offen offset:64 \n"
    "ds_read_b64_tr_b16 a[102:103], v34 offset:19200\n"
    "ds_read_b64_tr_b16 v[64:65], v61 offset:18432\n"
    "s_waitcnt lgkmcnt(5)\n"
    "v_mfma_f32_16x16x32_bf16 v[84:87], v[52:55], v[72:75], v[84:87]\n"
    "ds_read_b64_tr_b16 v[66:67], v61 offset:19200\n"
    "s_waitcnt vmcnt(24)\n"
    "s_barrier\n"
    "v_mfma_f32_16x16x32_bf16 v[88:91], v[54:57], v[72:75], v[88:91]\n"
    "buffer_load_dwordx4 v[220:223], v63, s[16:19], 0 offen offset:0 nt\n"
    "s_waitcnt lgkmcnt(4)\n"
    "ds_read_b64_tr_b16 a[104:105], v34 offset:18440\n"
    "v_mfma_f32_16x16x32_bf16 v[76:79], v[48:51], a[96:99], 0\n"
    "v_cvt_pk_bf16_f32 v96, v84, v85\n"
    "v_cvt_pk_bf16_f32 v97, v86, v87\n"
    "v_accvgpr_write_b32 a94, v96\n"
    "v_accvgpr_write_b32 a95, v97\n"
    "v_cvt_pk_bf16_f32 v98, v88, v89\n"
    "v_cvt_pk_bf16_f32 v99, v90, v91\n"
    "v_accvgpr_write_b32 a96, v98\n"
    "v_accvgpr_write_b32 a97, v99\n"
    "ds_read_b64_tr_b16 a[106:107], v32 offset:18440\n"
    "s_waitcnt lgkmcnt(4)\n"
    "buffer_load_dwordx4 v[224:227], v63, s[16:19], 0 offen offset:64 nt\n"
    "v_mfma_f32_16x16x32_bf16 v[80:83], v[48:51], a[100:103], 0\n"
    "ds_read_b64_tr_b16 a[108:109], v32 offset:19208\n"
    "ds_read_b64_tr_b16 a[110:111], v34 offset:19208\n"
    "s_waitcnt lgkmcnt(4)\n"
    "v_mfma_f32_16x16x32_bf16 v[76:79], v[52:55], v[64:67], v[76:79]\n"
    "ds_read_b64_tr_b16 v[68:69], v61 offset:18440\n"
    "v_mfma_f32_16x16x32_bf16 v[80:83], v[54:57], v[64:67], v[80:83]\n"
    "ds_read_b64_tr_b16 v[70:71], v61 offset:19208\n"
    "ds_read_b64_tr_b16 a[112:113], v35 offset:18432\n"
    "s_nop 3\n"
    "v_cvt_pk_bf16_f32 v92, v76, v77\n"
    "v_cvt_pk_bf16_f32 v93, v78, v79\n"
    "v_accvgpr_write_b32 a102, v92\n"
    "v_accvgpr_write_b32 a103, v93\n"
    "v_cvt_pk_bf16_f32 v94, v80, v81\n"
    "s_waitcnt lgkmcnt(5)\n"
    "v_cvt_pk_bf16_f32 v95, v82, v83\n"
    "ds_read_b64_tr_b16 a[114:115], v33 offset:18432\n"
    "v_mfma_f32_16x16x32_bf16 v[84:87], v[48:51], a[104:107], 0\n"
    "v_accvgpr_write_b32 a104, v94\n"
    "v_accvgpr_write_b32 a105, v95\n"
    "ds_read_b64_tr_b16 a[116:117], v33 offset:19200\n"
    "s_waitcnt lgkmcnt(5)\n"
    "ds_read_b64_tr_b16 a[118:119], v35 offset:19200\n"
    "v_mfma_f32_16x16x32_bf16 v[88:91], v[48:51], a[108:111], 0\n"
    "ds_read_b64_tr_b16 v[72:73], v62 offset:18432\n"
    "s_waitcnt lgkmcnt(5)\n"
    "ds_read_b64_tr_b16 v[74:75], v62 offset:19200\n"
    "v_mfma_f32_16x16x32_bf16 v[84:87], v[52:55], v[68:71], v[84:87]\n"
    "ds_read_b64_tr_b16 a[120:121], v34 offset:36864\n"
    "v_mfma_f32_16x16x32_bf16 v[88:91], v[54:57], v[68:71], v[88:91]\n"
    "ds_read_b64_tr_b16 a[122:123], v32 offset:36864\n"
    "s_waitcnt lgkmcnt(6)\n"
    "ds_read_b64_tr_b16 a[124:125], v32 offset:37632\n"
    "v_mfma_f32_16x16x32_bf16 v[76:79], v[48:51], a[112:115], 0\n"
    "s_nop 1\n"
    "v_cvt_pk_bf16_f32 v96, v84, v85\n"
    "v_cvt_pk_bf16_f32 v97, v86, v87\n"
    "v_cvt_pk_bf16_f32 v98, v88, v89\n"
    "v_cvt_pk_bf16_f32 v99, v90, v91\n"
    "v_accvgpr_write_b32 a110, v96\n"
    "v_accvgpr_write_b32 a111, v97\n"
    "s_waitcnt lgkmcnt(5)\n"
    "v_accvgpr_write_b32 a112, v98\n"
    "v_accvgpr_write_b32 a113, v99\n"
    "v_mfma_f32_16x16x32_bf16 v[80:83], v[48:51], a[116:119], 0\n"
    "ds_read_b64_tr_b16 a[126:127], v34 offset:37632\n"
    "ds_read_b64_tr_b16 v[64:65], v61 offset:36864\n"
    "s_waitcnt lgkmcnt(5)\n"
    "v_mfma_f32_16x16x32_bf16 v[76:79], v[52:55], v[72:75], v[76:79]\n"
    "ds_read_b64_tr_b16 v[66:67], v61 offset:37632\n"
    "s_waitcnt vmcnt(21)\n"
    "s_barrier\n"
    "v_mfma_f32_16x16x32_bf16 v[80:83], v[54:57], v[72:75], v[80:83]\n"
    "s_waitcnt lgkmcnt(4)\n"
    "ds_read_b64_tr_b16 a[128:129], v34 offset:36872\n"
    "v_mfma_f32_16x16x32_bf16 v[84:87], v[48:51], a[120:123], 0\n"
    "ds_read_b64_tr_b16 a[130:131], v32 offset:36872\n"
    "v_cvt_pk_bf16_f32 v92, v76, v77\n"
    "v_cvt_pk_bf16_f32 v93, v78, v79\n"
    "v_accvgpr_write_b32 a118, v92\n"
    "v_accvgpr_write_b32 a119, v93\n"
    "v_cvt_pk_bf16_f32 v94, v80, v81\n"
    "v_cvt_pk_bf16_f32 v95, v82, v83\n"
    "v_accvgpr_write_b32 a120, v94\n"
    "v_accvgpr_write_b32 a121, v95\n"
    "ds_read_b64_tr_b16 a[132:133], v32 offset:37640\n"
    "v_add_u32 v32, 0xd800, v32\n"
    "s_waitcnt lgkmcnt(5)\n"
    "ds_read_b64_tr_b16 a[134:135], v34 offset:37640\n"
    "v_mfma_f32_16x16x32_bf16 v[88:91], v[48:51], a[124:127], 0\n"
    "ds_read_b64_tr_b16 v[68:69], v61 offset:36872\n"
    "v_add_u32 v34, 0xd800, v34\n"
    "s_waitcnt lgkmcnt(5)\n"
    "ds_read_b64_tr_b16 v[70:71], v61 offset:37640\n"
    "v_mfma_f32_16x16x32_bf16 v[84:87], v[52:55], v[64:67], v[84:87]\n"
    "ds_read_b64_tr_b16 a[136:137], v35 offset:36864\n"
    "v_mfma_f32_16x16x32_bf16 v[88:91], v[54:57], v[64:67], v[88:91]\n"
    "ds_read_b64_tr_b16 a[138:139], v33 offset:36864\n"
    "s_waitcnt lgkmcnt(6)\n"
    "v_add_u32 v61, 0xd800, v61\n"
    "v_mfma_f32_16x16x32_bf16 v[76:79], v[48:51], a[128:131], 0\n"
    "ds_read_b64_tr_b16 a[140:141], v33 offset:37632\n"
    "s_nop 0\n"
    "v_cvt_pk_bf16_f32 v96, v84, v85\n"
    "v_cvt_pk_bf16_f32 v97, v86, v87\n"
    "v_accvgpr_write_b32 a126, v96\n"
    "v_cvt_pk_bf16_f32 v98, v88, v89\n"
    "v_cvt_pk_bf16_f32 v99, v90, v91\n"
    "v_accvgpr_write_b32 a127, v97\n"
    "v_accvgpr_write_b32 a128, v98\n"
    "v_accvgpr_write_b32 a129, v99\n"
    "s_waitcnt lgkmcnt(5)\n"
    "ds_read_b64_tr_b16 a[142:143], v35 offset:37632\n"
    "v_mfma_f32_16x16x32_bf16 v[80:83], v[48:51], a[132:135], 0\n"
    "ds_read_b64_tr_b16 v[72:73], v62 offset:36864\n"
    "v_add_u32 v33, 0xd800, v33\n"
    "v_add_u32 v35, 0xd800, v35\n"
    "s_waitcnt lgkmcnt(5)\n"
    "ds_read_b64_tr_b16 v[74:75], v62 offset:37632\n"
    "v_mfma_f32_16x16x32_bf16 v[76:79], v[52:55], v[68:71], v[76:79]\n"
    "v_add_u32 v62, 0xd800, v62\n"
    "v_mfma_f32_16x16x32_bf16 v[80:83], v[54:57], v[68:71], v[80:83]\n"
    "s_waitcnt lgkmcnt(4)\n"
    "ds_read_b64_tr_b16 a[144:145], v34 offset:0\n"
    "v_mfma_f32_16x16x32_bf16 v[84:87], v[48:51], a[136:139], 0\n"
    "ds_read_b64_tr_b16 a[146:147], v32 offset:0\n"
    "s_nop 1\n"
    "v_cvt_pk_bf16_f32 v92, v76, v77\n"
    "v_cvt_pk_bf16_f32 v93, v78, v79\n"
    "v_accvgpr_write_b32 a134, v92\n"
    "v_accvgpr_write_b32 a135, v93\n"
    "v_cvt_pk_bf16_f32 v94, v80, v81\n"
    "v_cvt_pk_bf16_f32 v95, v82, v83\n"
    "v_accvgpr_write_b32 a136, v94\n"
    "v_accvgpr_write_b32 a137, v95\n"
    "ds_read_b64_tr_b16 a[148:149], v32 offset:768\n"
    "s_waitcnt lgkmcnt(5)\n"
    "v_mfma_f32_16x16x32_bf16 v[88:91], v[48:51], a[140:143], 0\n"
    "ds_read_b64_tr_b16 a[150:151], v34 offset:768\n"
    "ds_read_b64_tr_b16 v[64:65], v61 offset:0\n"
    "s_waitcnt lgkmcnt(5)\n"
    "v_mfma_f32_16x16x32_bf16 v[84:87], v[52:55], v[72:75], v[84:87]\n"
    "ds_read_b64_tr_b16 v[66:67], v61 offset:768\n"
    "s_waitcnt vmcnt(16)\n"
    "s_barrier\n"
    "v_mfma_f32_16x16x32_bf16 v[88:91], v[54:57], v[72:75], v[88:91]\n"
    "ds_read_b64_tr_b16 a[152:153], v34 offset:8\n"
    "s_waitcnt lgkmcnt(5)\n"
    "ds_read_b64_tr_b16 a[154:155], v32 offset:8\n"
    "v_mfma_f32_16x16x32_bf16 v[76:79], v[48:51], a[144:147], 0\n"
    "v_cvt_pk_bf16_f32 v96, v84, v85\n"
    "v_cvt_pk_bf16_f32 v97, v86, v87\n"
    "s_nop 1\n"
    "v_cvt_pk_bf16_f32 v98, v88, v89\n"
    "v_cvt_pk_bf16_f32 v99, v90, v91\n"
    "v_accvgpr_write_b32 a142, v96\n"
    "v_accvgpr_write_b32 a143, v97\n"
    "v_accvgpr_write_b32 a144, v98\n"
    "v_accvgpr_write_b32 a145, v99\n"
    "ds_read_b64_tr_b16 a[156:157], v32 offset:776\n"
    "s_waitcnt lgkmcnt(5)\n"
    "v_add_u32 v32, 0xfffeda00, v32\n"
    "v_mfma_f32_16x16x32_bf16 v[80:83], v[48:51], a[148:151], 0\n"
    "ds_read_b64_tr_b16 a[158:159], v34 offset:776\n"
    "v_add_u32 v34, 0xfffeda00, v34\n"
    "ds_read_b64_tr_b16 v[68:69], v61 offset:8\n"
    "s_waitcnt lgkmcnt(5)\n"
    "v_mfma_f32_16x16x32_bf16 v[76:79], v[52:55], v[64:67], v[76:79]\n"
    "ds_read_b64_tr_b16 v[70:71], v61 offset:776\n"
    "v_add_u32 v61, 0xfffeda00, v61\n"
    "v_mfma_f32_16x16x32_bf16 v[80:83], v[54:57], v[64:67], v[80:83]\n"
    "ds_read_b64_tr_b16 a[160:161], v35 offset:0\n"
    "s_waitcnt lgkmcnt(5)\n"
    "ds_read_b64_tr_b16 a[162:163], v33 offset:0\n"
    "v_mfma_f32_16x16x32_bf16 v[84:87], v[48:51], a[152:155], 0\n"
    "s_nop 0\n"
    "v_cvt_pk_bf16_f32 v92, v76, v77\n"
    "v_cvt_pk_bf16_f32 v93, v78, v79\n"
    "v_accvgpr_write_b32 a150, v92\n"
    "v_cvt_pk_bf16_f32 v94, v80, v81\n"
    "v_cvt_pk_bf16_f32 v95, v82, v83\n"
    "v_accvgpr_write_b32 a151, v93\n"
    "v_accvgpr_write_b32 a152, v94\n"
    "v_accvgpr_write_b32 a153, v95\n"
    "ds_read_b64_tr_b16 a[164:165], v33 offset:768\n"
    "v_add_u32 v33, 0xfffeda00, v33\n"
    "s_waitcnt lgkmcnt(5)\n"
    "ds_read_b64_tr_b16 a[166:167], v35 offset:768\n"
    "v_mfma_f32_16x16x32_bf16 v[88:91], v[48:51], a[156:159], 0\n"
    "ds_read_b64_tr_b16 v[72:73], v62 offset:0\n"
    "v_add_u32 v35, 0xfffeda00, v35\n"
    "s_waitcnt lgkmcnt(5)\n"
    "ds_read_b64_tr_b16 v[74:75], v62 offset:768\n"
    "v_mfma_f32_16x16x32_bf16 v[84:87], v[52:55], v[68:71], v[84:87]\n"
    "v_add_u32 v62, 0xfffeda00, v62\n"
    "v_mfma_f32_16x16x32_bf16 v[88:91], v[54:57], v[68:71], v[88:91]\n"
    "s_waitcnt lgkmcnt(4)\n"
    "ds_read_b64_tr_b16 a[168:169], v34 offset:0\n"
    "v_mfma_f32_16x16x32_bf16 v[76:79], v[48:51], a[160:163], 0\n"
    "ds_read_b64_tr_b16 a[170:171], v32 offset:0\n"
    "s_nop 1\n"
    "v_cvt_pk_bf16_f32 v96, v84, v85\n"
    "v_cvt_pk_bf16_f32 v97, v86, v87\n"
    "v_accvgpr_write_b32 a158, v96\n"
    "v_accvgpr_write_b32 a159, v97\n"
    "v_cvt_pk_bf16_f32 v98, v88, v89\n"
    "v_cvt_pk_bf16_f32 v99, v90, v91\n"
    "v_accvgpr_write_b32 a160, v98\n"
    "v_accvgpr_write_b32 a161, v99\n"
    "ds_read_b64_tr_b16 a[172:173], v32 offset:768\n"
    "s_waitcnt lgkmcnt(5)\n"
    "v_mfma_f32_16x16x32_bf16 v[80:83], v[48:51], a[164:167], 0\n"
    "ds_read_b64_tr_b16 a[174:175], v34 offset:768\n"
    "ds_read_b64_tr_b16 v[64:65], v61 offset:0\n"
    "s_waitcnt lgkmcnt(5)\n"
    "v_mfma_f32_16x16x32_bf16 v[76:79], v[52:55], v[72:75], v[76:79]\n"
    "ds_read_b64_tr_b16 v[66:67], v61 offset:768\n"
    "s_waitcnt vmcnt(11)\n"
    "s_barrier\n"
    "v_mfma_f32_16x16x32_bf16 v[80:83], v[54:57], v[72:75], v[80:83]\n"
    "ds_read_b64_tr_b16 a[176:177], v34 offset:8\n"
    "s_waitcnt lgkmcnt(5)\n"
    "ds_read_b64_tr_b16 a[178:179], v32 offset:8\n"
    "v_mfma_f32_16x16x32_bf16 v[84:87], v[48:51], a[168:171], 0\n"
    "v_cvt_pk_bf16_f32 v92, v76, v77\n"
    "v_cvt_pk_bf16_f32 v93, v78, v79\n"
    "s_nop 1\n"
    "v_cvt_pk_bf16_f32 v94, v80, v81\n"
    "v_cvt_pk_bf16_f32 v95, v82, v83\n"
    "v_accvgpr_write_b32 a166, v92\n"
    "v_accvgpr_write_b32 a167, v93\n"
    "v_accvgpr_write_b32 a168, v94\n"
    "v_accvgpr_write_b32 a169, v95\n"
    "ds_read_b64_tr_b16 a[180:181], v32 offset:776\n"
    "s_waitcnt lgkmcnt(5)\n"
    "v_add_u32 v32, 0xffffb800, v32\n"
    "v_mfma_f32_16x16x32_bf16 v[88:91], v[48:51], a[172:175], 0\n"
    "ds_read_b64_tr_b16 a[182:183], v34 offset:776\n"
    "v_add_u32 v34, 0xffffb800, v34\n"
    "ds_read_b64_tr_b16 v[68:69], v61 offset:8\n"
    "s_waitcnt lgkmcnt(5)\n"
    "v_mfma_f32_16x16x32_bf16 v[84:87], v[52:55], v[64:67], v[84:87]\n"
    "ds_read_b64_tr_b16 v[70:71], v61 offset:776\n"
    "v_add_u32 v61, 0xffffb800, v61\n"
    "v_mfma_f32_16x16x32_bf16 v[88:91], v[54:57], v[64:67], v[88:91]\n"
    "ds_read_b64_tr_b16 a[184:185], v35 offset:0\n"
    "s_waitcnt lgkmcnt(5)\n"
    "ds_read_b64_tr_b16 a[186:187], v33 offset:0\n"
    "v_mfma_f32_16x16x32_bf16 v[76:79], v[48:51], a[176:179], 0\n"
    "s_nop 0\n"
    "v_cvt_pk_bf16_f32 v96, v84, v85\n"
    "v_cvt_pk_bf16_f32 v97, v86, v87\n"
    "v_accvgpr_write_b32 a174, v96\n"
    "v_cvt_pk_bf16_f32 v98, v88, v89\n"
    "v_cvt_pk_bf16_f32 v99, v90, v91\n"
    "v_accvgpr_write_b32 a175, v97\n"
    "v_accvgpr_write_b32 a176, v98\n"
    "v_accvgpr_write_b32 a177, v99\n"
    "ds_read_b64_tr_b16 a[188:189], v33 offset:768\n"
    "v_add_u32 v33, 0xffffb800, v33\n"
    "s_waitcnt lgkmcnt(5)\n"
    "ds_read_b64_tr_b16 a[190:191], v35 offset:768\n"
    "v_mfma_f32_16x16x32_bf16 v[80:83], v[48:51], a[180:183], 0\n"
    "ds_read_b64_tr_b16 v[72:73], v62 offset:0\n"
    "v_add_u32 v35, 0xffffb800, v35\n"
    "s_waitcnt lgkmcnt(5)\n"
    "ds_read_b64_tr_b16 v[74:75], v62 offset:768\n"
    "v_mfma_f32_16x16x32_bf16 v[76:79], v[52:55], v[68:71], v[76:79]\n"
    "v_add_u32 v62, 0xffffb800, v62\n"
    "v_mfma_f32_16x16x32_bf16 v[80:83], v[54:57], v[68:71], v[80:83]\n"
    "s_waitcnt lgkmcnt(4)\n"
    "ds_read_b64_tr_b16 a[192:193], v34 offset:0\n"
    "v_mfma_f32_16x16x32_bf16 v[84:87], v[48:51], a[184:187], 0\n"
    "ds_read_b64_tr_b16 a[194:195], v32 offset:0\n"
    "s_nop 1\n"
    "v_cvt_pk_bf16_f32 v92, v76, v77\n"
    "v_cvt_pk_bf16_f32 v93, v78, v79\n"
    "v_accvgpr_write_b32 a182, v92\n"
    "v_accvgpr_write_b32 a183, v93\n"
    "v_cvt_pk_bf16_f32 v94, v80, v81\n"
    "v_cvt_pk_bf16_f32 v95, v82, v83\n"
    "v_accvgpr_write_b32 a184, v94\n"
    "v_accvgpr_write_b32 a185, v95\n"
    "ds_read_b64_tr_b16 a[196:197], v32 offset:768\n"
    "s_waitcnt lgkmcnt(5)\n"
    "buffer_load_dwordx4 v[240:243], v63, s[16:19], 0 offen offset:128 nt\n"
    "v_mfma_f32_16x16x32_bf16 v[88:91], v[48:51], a[188:191], 0\n"
    "ds_read_b64_tr_b16 a[198:199], v34 offset:768\n"
    "s_waitcnt lgkmcnt(4)\n"
    "ds_read_b64_tr_b16 v[64:65], v61 offset:0\n"
    "v_mfma_f32_16x16x32_bf16 v[84:87], v[52:55], v[72:75], v[84:87]\n"
    "ds_read_b64_tr_b16 v[66:67], v61 offset:768\n"
    "v_mfma_f32_16x16x32_bf16 v[88:91], v[54:57], v[72:75], v[88:91]\n"
    "ds_read_b64_tr_b16 a[200:201], v34 offset:8\n"
    "s_waitcnt lgkmcnt(5)\n"
    "ds_read_b64_tr_b16 a[202:203], v32 offset:8\n"
    "v_mfma_f32_16x16x32_bf16 v[76:79], v[48:51], a[192:195], 0\n"
    "s_nop 1\n"
    "v_cvt_pk_bf16_f32 v96, v84, v85\n"
    "v_cvt_pk_bf16_f32 v97, v86, v87\n"
    "v_cvt_pk_bf16_f32 v98, v88, v89\n"
    "v_cvt_pk_bf16_f32 v99, v90, v91\n"
    "v_accvgpr_write_b32 a190, v96\n"
    "v_accvgpr_write_b32 a191, v97\n"
    "v_accvgpr_write_b32 a192, v98\n"
    "v_accvgpr_write_b32 a193, v99\n"
    "ds_read_b64_tr_b16 a[204:205], v32 offset:776\n"
    "s_waitcnt lgkmcnt(5)\n"
    "ds_read_b64_tr_b16 a[206:207], v34 offset:776\n"
    "v_mfma_f32_16x16x32_bf16 v[80:83], v[48:51], a[196:199], 0\n"
    "ds_read_b64_tr_b16 v[68:69], v61 offset:8\n"
    "s_waitcnt lgkmcnt(5)\n"
    "ds_read_b64_tr_b16 v[70:71], v61 offset:776\n"
    "v_mfma_f32_16x16x32_bf16 v[76:79], v[52:55], v[64:67], v[76:79]\n"
    "buffer_load_dwordx4 v[184:187], v47, s[20:23], 0 offen offset:0 nt\n"
    "v_mfma_f32_16x16x32_bf16 v[80:83], v[54:57], v[64:67], v[80:83]\n"
    "s_waitcnt lgkmcnt(4)\n"
    "ds_read_b64_tr_b16 a[208:209], v35 offset:0\n"
    "v_mfma_f32_16x16x32_bf16 v[84:87], v[48:51], a[200:203], 0\n"
    "ds_read_b64_tr_b16 a[210:211], v33 offset:0\n"
    "s_nop 1\n"
    "v_cvt_pk_bf16_f32 v92, v76, v77\n"
    "v_cvt_pk_bf16_f32 v93, v78, v79\n"
    "v_accvgpr_write_b32 a198, v92\n"
    "v_accvgpr_write_b32 a199, v93\n"
    "v_cvt_pk_bf16_f32 v94, v80, v81\n"
    "v_cvt_pk_bf16_f32 v95, v82, v83\n"
    "v_accvgpr_write_b32 a200, v94\n"
    "v_accvgpr_write_b32 a201, v95\n"
    "ds_read_b64_tr_b16 a[212:213], v33 offset:768\n"
    "s_waitcnt lgkmcnt(5)\n"
    "v_mfma_f32_16x16x32_bf16 v[88:91], v[48:51], a[204:207], 0\n"
    "ds_read_b64_tr_b16 a[214:215], v35 offset:768\n"
    "ds_read_b64_tr_b16 v[72:73], v62 offset:0\n"
    "s_waitcnt lgkmcnt(5)\n"
    "v_mfma_f32_16x16x32_bf16 v[84:87], v[52:55], v[68:71], v[84:87]\n"
    "ds_read_b64_tr_b16 v[74:75], v62 offset:768\n"
    "v_mfma_f32_16x16x32_bf16 v[88:91], v[54:57], v[68:71], v[88:91]\n"
    "s_waitcnt lgkmcnt(4)\n"
    "v_mfma_f32_16x16x32_bf16 v[76:79], v[48:51], a[208:211], 0\n"
    "s_nop 3\n"
    "v_cvt_pk_bf16_f32 v96, v84, v85\n"
    "v_cvt_pk_bf16_f32 v97, v86, v87\n"
    "v_cvt_pk_bf16_f32 v98, v88, v89\n"
    "v_cvt_pk_bf16_f32 v99, v90, v91\n"
    "buffer_load_dwordx2 v[188:189], v60, s[20:23], 0 offen offset:64 nt\n"
    "v_accvgpr_write_b32 a206, v96\n"
    "v_accvgpr_write_b32 a207, v97\n"
    "v_accvgpr_write_b32 a208, v98\n"
    "s_waitcnt lgkmcnt(2)\n"
    "v_accvgpr_write_b32 a209, v99\n"
    "v_mfma_f32_16x16x32_bf16 v[80:83], v[48:51], a[212:215], 0\n"
    "s_waitcnt lgkmcnt(0)\n"
    "v_mfma_f32_16x16x32_bf16 v[76:79], v[52:55], v[72:75], v[76:79]\n"
    "v_mfma_f32_16x16x32_bf16 v[80:83], v[54:57], v[72:75], v[80:83]\n"
    "s_nop 6\n"
    "v_cvt_pk_bf16_f32 v92, v76, v77\n"
    "v_cvt_pk_bf16_f32 v93, v78, v79\n"
    "v_accvgpr_write_b32 a214, v92\n"
    "v_accvgpr_write_b32 a215, v93\n"
    "v_cvt_pk_bf16_f32 v94, v80, v81\n"
    "v_cvt_pk_bf16_f32 v95, v82, v83\n"
    "v_accvgpr_write_b32 a216, v94\n"
    "v_accvgpr_write_b32 a217, v95\n"
    "s_add_u32 m0, s45, 0x20400\n"
    "s_add_u32 s52, s48, 0x12000\n"
    "buffer_load_dwordx4 v18, s[16:19], s52 offen nt lds\n"
    "s_add_u32 m0, s45, 0x21400\n"
    "s_add_u32 s53, s48, 0x12000\n"
    "buffer_load_dwordx4 v19, s[16:19], s53 offen nt lds\n"
    "s_waitcnt vmcnt(11)\n"
    "s_barrier\n"
    "ds_read_b128 v[100:103], v6 offset:0\n"
    "ds_read_b128 v[104:107], v6 offset:64\n"
    "ds_read_b128 v[108:111], v6 offset:128\n"
    "ds_read_b128 v[112:115], v6 offset:9216\n"
    "ds_read_b128 v[116:119], v6 offset:9280\n"
    "ds_read_b128 v[120:123], v6 offset:9344\n"
    "s_add_u32 m0, s45, 0x22400\n"
    "s_add_u32 s54, s48, 0x12000\n"
    "buffer_load_dwordx4 v20, s[16:19], s54 offen nt lds\n"
    "s_waitcnt vmcnt(11) lgkmcnt(3)\n"
    "v_lshlrev_b32 v124, 16, v196\n"
    "v_and_b32 v125, 0xffff0000, v196\n"
    "v_lshlrev_b32 v126, 16, v197\n"
    "v_and_b32 v127, 0xffff0000, v197\n"
    "v_lshlrev_b32 v128, 16, v198\n"
    "v_and_b32 v129, 0xffff0000, v198\n"
    "v_mfma_f32_16x16x32_bf16 v[124:127], a[220:223], v[100:103], v[124:127]\n"
    "s_waitcnt lgkmcnt(0)\n"
    "v_lshlrev_b32 v130, 16, v199\n"
    "v_and_b32 v131, 0xffff0000, v199\n"
    "s_nop 1\n"
    "v_mfma_f32_16x16x32_bf16 v[128:131], a[224:227], v[100:103], v[128:131]\n"
    "s_add_u32 m0, s45, 0x23400\n"
    "s_add_u32 s55, s48, 0x12000\n"
    "buffer_load_dwordx4 v21, s[16:19], s55 offen nt lds\n"
    "s_waitcnt vmcnt(11)\n"
    "v_lshlrev_b32 v132, 16, v200\n"
    "v_and_b32 v133, 0xffff0000, v200\n"
    "v_lshlrev_b32 v134, 16, v201\n"
    "v_and_b32 v135, 0xffff0000, v201\n"
    "s_nop 1\n"
    "v_mfma_f32_16x16x32_bf16 v[132:135], a[228:231], v[100:103], v[132:135]\n"
    "ds_read_b128 v[100:103], v7 offset:0\n"
    "v_mfma_f32_16x16x32_bf16 v[124:127], a[232:235], v[104:107], v[124:127]\n"
    "v_mfma_f32_16x16x32_bf16 v[128:131], a[236:239], v[104:107], v[128:131]\n"
    "v_mfma_f32_16x16x32_bf16 v[132:135], a[240:243], v[104:107], v[132:135]\n"
    "ds_read_b128 v[104:107], v7 offset:64\n"
    "v_mfma_f32_16x16x32_bf16 v[124:127], a[244:247], v[108:111], v[124:127]\n"
    "v_mfma_f32_16x16x32_bf16 v[128:131], a[248:251], v[108:111], v[128:131]\n"
    "v_mfma_f32_16x16x32_bf16 v[132:135], a[252:255], v[108:111], v[132:135]\n"
    "ds_read_b128 v[108:111], v7 offset:128\n"
    "s_add_u32 m0, s46, 0x24400\n"
    "s_add_u32 s52, s48, 0x12000\n"
    "s_mov_b32 exec_hi, 0\n"
    "buffer_load_dwordx4 v22, s[16:19], s52 offen nt lds\n"
    "s_mov_b32 exec_hi, -1\n"
    "s_waitcnt vmcnt(11) lgkmcnt(2)\n"
    "v_lshlrev_b32 v148, 16, v202\n"
    "v_and_b32 v149, 0xffff0000, v202\n"
    "v_lshlrev_b32 v150, 16, v203\n"
    "v_and_b32 v151, 0xffff0000, v203\n"
    "v_lshlrev_b32 v152, 16, v204\n"
    "v_and_b32 v153, 0xffff0000, v204\n"
    "v_mfma_f32_16x16x32_bf16 v[136:139], a[220:223], v[112:115], v[148:151]\n"
    "v_lshlrev_b32 v154, 16, v205\n"
    "v_and_b32 v155, 0xffff0000, v205\n"
    "s_nop 1\n"
    "v_mfma_f32_16x16x32_bf16 v[140:143], a[224:227], v[112:115], v[152:155]\n"
    "s_waitcnt lgkmcnt(1)\n"
    "s_add_u32 s53, s49, 0x9000\n"
    "buffer_load_dwordx4 v[190:193], v23, s[20:23], s53 offen offset:0 \n"
    "s_waitcnt vmcnt(11) lgkmcnt(0)\n"
    "v_lshlrev_b32 v156, 16, v206\n"
    "v_and_b32 v157, 0xffff0000, v206\n"
    "v_lshlrev_b32 v158, 16, v207\n"
    "v_and_b32 v159, 0xffff0000, v207\n"
    "s_nop 1\n"
    "v_mfma_f32_16x16x32_bf16 v[144:147], a[228:231], v[112:115], v[156:159]\n"
    "v_mfma_f32_16x16x32_bf16 v[136:139], a[232:235], v[116:119], v[136:139]\n"
    "v_mfma_f32_16x16x32_bf16 v[140:143], a[236:239], v[116:119], v[140:143]\n"
    "v_mfma_f32_16x16x32_bf16 v[144:147], a[240:243], v[116:119], v[144:147]\n"
    "v_mfma_f32_16x16x32_bf16 v[136:139], a[244:247], v[120:123], v[136:139]\n"
    "v_mfma_f32_16x16x32_bf16 v[140:143], a[248:251], v[120:123], v[140:143]\n"
    "v_mfma_f32_16x16x32_bf16 v[144:147], a[252:255], v[120:123], v[144:147]\n"
    "s_add_u32 s54, s49, 0x9000\n"
    "buffer_load_dwordx2 v[194:195], v24, s[20:23], s54 offen offset:64 \n"
    "buffer_load_dwordx4 v[208:211], v45, s[20:23], 0 offen offset:0 \n"
    "buffer_load_dwordx2 v[212:213], v46, s[20:23], 0 offen offset:64 \n"
    "buffer_load_dwordx4 v[214:217], v25, s[20:23], s49 offen offset:0 \n"
    "s_waitcnt vmcnt(11)\n"
    "v_lshlrev_b32 v172, 16, v184\n"
    "v_and_b32 v173, 0xffff0000, v184\n"
    "v_lshlrev_b32 v174, 16, v185\n"
    "v_and_b32 v175, 0xffff0000, v185\n"
    "v_lshlrev_b32 v176, 16, v186\n"
    "v_and_b32 v177, 0xffff0000, v186\n"
    "v_mfma_f32_16x16x32_bf16 v[172:175], a[220:223], v[220:223], v[172:175]\n"
    "v_lshlrev_b32 v178, 16, v187\n"
    "v_and_b32 v179, 0xffff0000, v187\n"
    "s_nop 1\n"
    "v_mfma_f32_16x16x32_bf16 v[176:179], a[224:227], v[220:223], v[176:179]\n"
    "buffer_load_dwordx2 v[218:219], v26, s[20:23], s49 offen offset:64 \n"
    "s_waitcnt vmcnt(11)\n"
    "v_lshlrev_b32 v180, 16, v188\n"
    "v_and_b32 v181, 0xffff0000, v188\n"
    "v_lshlrev_b32 v182, 16, v189\n"
    "v_and_b32 v183, 0xffff0000, v189\n"
    "s_nop 1\n"
    "v_mfma_f32_16x16x32_bf16 v[180:183], a[228:231], v[220:223], v[180:183]\n"
    "v_mfma_f32_16x16x32_bf16 v[172:175], a[232:235], v[224:227], v[172:175]\n"
    "v_mfma_f32_16x16x32_bf16 v[176:179], a[236:239], v[224:227], v[176:179]\n"
    "v_mfma_f32_16x16x32_bf16 v[180:183], a[240:243], v[224:227], v[180:183]\n"
    "v_mfma_f32_16x16x32_bf16 v[172:175], a[244:247], v[240:243], v[172:175]\n"
    "v_mfma_f32_16x16x32_bf16 v[176:179], a[248:251], v[240:243], v[176:179]\n"
    "v_mfma_f32_16x16x32_bf16 v[180:183], a[252:255], v[240:243], v[180:183]\n"
    "s_nop 5\n"
    "v_subrev_f32_dpp v220, v172, v172 row_shl:2 row_mask:0xf bank_mask:0xf bound_ctrl:0\n"
    "v_subrev_f32_dpp v221, v173, v173 row_shl:2 row_mask:0xf bank_mask:0xf bound_ctrl:0\n"
    "v_cvt_pk_bf16_f32 v228, v220, v221\n"
    "v_subrev_f32_dpp v222, v174, v174 row_shl:2 row_mask:0xf bank_mask:0xf bound_ctrl:0\n"
    "v_subrev_f32_dpp v223, v175, v175 row_shl:2 row_mask:0xf bank_mask:0xf bound_ctrl:0\n"
    "v_cvt_pk_bf16_f32 v229, v222, v223\n"
    "v_subrev_f32_dpp v224, v176, v176 row_shl:2 row_mask:0xf bank_mask:0xf bound_ctrl:0\n"
    "v_subrev_f32_dpp v225, v177, v177 row_shl:2 row_mask:0xf bank_mask:0xf bound_ctrl:0\n"
    "v_cvt_pk_bf16_f32 v230, v224, v225\n"
    "v_subrev_f32_dpp v226, v178, v178 row_shl:2 row_mask:0xf bank_mask:0xf bound_ctrl:0\n"
    "v_subrev_f32_dpp v227, v179, v179 row_shl:2 row_mask:0xf bank_mask:0xf bound_ctrl:0\n"
    "v_cvt_pk_bf16_f32 v231, v226, v227\n"
    "v_subrev_f32_dpp v220, v180, v180 row_shl:2 row_mask:0xf bank_mask:0xf bound_ctrl:0\n"
    "v_subrev_f32_dpp v221, v181, v181 row_shl:2 row_mask:0xf bank_mask:0xf bound_ctrl:0\n"
    "v_cvt_pk_bf16_f32 v232, v220, v221\n"
    "v_subrev_f32_dpp v222, v182, v182 row_shl:2 row_mask:0xf bank_mask:0xf bound_ctrl:0\n"
    "v_subrev_f32_dpp v223, v183, v183 row_shl:2 row_mask:0xf bank_mask:0xf bound_ctrl:0\n"
    "v_cvt_pk_bf16_f32 v233, v222, v223\n"
    "v_add_f32_dpp v240, v172, v172 row_shl:1 row_mask:0xf bank_mask:0xf bound_ctrl:0\n"
    "v_subrev_f32_dpp v224, v172, v172 row_shr:1 row_mask:0xf bank_mask:0xf bound_ctrl:0\n"
    "v_cndmask_b32_e64 v224, v224, v240, s[50:51]\n"
    "v_add_f32_dpp v241, v173, v173 row_shl:1 row_mask:0xf bank_mask:0xf bound_ctrl:0\n"
    "v_subrev_f32_dpp v225, v173, v173 row_shr:1 row_mask:0xf bank_mask:0xf bound_ctrl:0\n"
    "v_cndmask_b32_e64 v225, v225, v241, s[50:51]\n"
    "v_cvt_pk_bf16_f32 v234, v224, v225\n"
    "v_add_f32_dpp v240, v174, v174 row_shl:1 row_mask:0xf bank_mask:0xf bound_ctrl:0\n"
    "v_subrev_f32_dpp v226, v174, v174 row_shr:1 row_mask:0xf bank_mask:0xf bound_ctrl:0\n"
    "v_cndmask_b32_e64 v226, v226, v240, s[50:51]\n"
    "v_add_f32_dpp v241, v175, v175 row_shl:1 row_mask:0xf bank_mask:0xf bound_ctrl:0\n"
    "v_subrev_f32_dpp v227, v175, v175 row_shr:1 row_mask:0xf bank_mask:0xf bound_ctrl:0\n"
    "v_cndmask_b32_e64 v227, v227, v241, s[50:51]\n"
    "v_cvt_pk_bf16_f32 v235, v226, v227\n"
    "v_add_f32_dpp v240, v176, v176 row_shl:1 row_mask:0xf bank_mask:0xf bound_ctrl:0\n"
    "v_subrev_f32_dpp v220, v176, v176 row_shr:1 row_mask:0xf bank_mask:0xf bound_ctrl:0\n"
    "v_cndmask_b32_e64 v220, v220, v240, s[50:51]\n"
    "v_add_f32_dpp v241, v177, v177 row_shl:1 row_mask:0xf bank_mask:0xf bound_ctrl:0\n"
    "v_subrev_f32_dpp v221, v177, v177 row_shr:1 row_mask:0xf bank_mask:0xf bound_ctrl:0\n"
    "v_cndmask_b32_e64 v221, v221, v241, s[50:51]\n"
    "v_cvt_pk_bf16_f32 v236, v220, v221\n"
    "v_add_f32_dpp v240, v178, v178 row_shl:1 row_mask:0xf bank_mask:0xf bound_ctrl:0\n"
    "v_subrev_f32_dpp v222, v178, v178 row_shr:1 row_mask:0xf bank_mask:0xf bound_ctrl:0\n"
    "v_cndmask_b32_e64 v222, v222, v240, s[50:51]\n"
    "v_add_f32_dpp v241, v179, v179 row_shl:1 row_mask:0xf bank_mask:0xf bound_ctrl:0\n"
    "v_subrev_f32_dpp v223, v179, v179 row_shr:1 row_mask:0xf bank_mask:0xf bound_ctrl:0\n"
    "v_cndmask_b32_e64 v223, v223, v241, s[50:51]\n"
    "v_cvt_pk_bf16_f32 v237, v222, v223\n"
    "v_add_f32_dpp v240, v180, v180 row_shl:1 row_mask:0xf bank_mask:0xf bound_ctrl:0\n"
    "v_subrev_f32_dpp v224, v180, v180 row_shr:1 row_mask:0xf bank_mask:0xf bound_ctrl:0\n"
    "v_cndmask_b32_e64 v224, v224, v240, s[50:51]\n"
    "v_add_f32_dpp v241, v181, v181 row_shl:1 row_mask:0xf bank_mask:0xf bound_ctrl:0\n"
    "v_subrev_f32_dpp v225, v181, v181 row_shr:1 row_mask:0xf bank_mask:0xf bound_ctrl:0\n"
    "v_cndmask_b32_e64 v225, v225, v241, s[50:51]\n"
    "v_cvt_pk_bf16_f32 v238, v224, v225\n"
    "v_add_f32_dpp v240, v182, v182 row_shl:1 row_mask:0xf bank_mask:0xf bound_ctrl:0\n"
    "v_subrev_f32_dpp v226, v182, v182 row_shr:1 row_mask:0xf bank_mask:0xf bound_ctrl:0\n"
    "v_cndmask_b32_e64 v226, v226, v240, s[50:51]\n"
    "v_add_f32_dpp v241, v183, v183 row_shl:1 row_mask:0xf bank_mask:0xf bound_ctrl:0\n"
    "v_subrev_f32_dpp v227, v183, v183 row_shr:1 row_mask:0xf bank_mask:0xf bound_ctrl:0\n"
    "v_cndmask_b32_e64 v227, v227, v241, s[50:51]\n"
    "v_cvt_pk_bf16_f32 v239, v226, v227\n"
    "s_add_u32 s53, s49, 0x1b000\n"
    "buffer_load_dwordx4 v[196:199], v23, s[20:23], s53 offen offset:0 \n"
    "s_add_u32 s54, s49, 0x1b000\n"
    "buffer_load_dwordx2 v[200:201], v24, s[20:23], s54 offen offset:64 \n"
    "s_waitcnt vmcnt(8)\n"
    "s_barrier\n"
    "ds_read_b128 v[112:115], v6 offset:18432\n"
    "ds_read_b128 v[116:119], v6 offset:18496\n"
    "ds_read_b128 v[120:123], v6 offset:18560\n"
    "s_add_u32 m0, s45, 0x1bc00\n"
    "s_add_u32 s55, s48, 0x36000\n"
    "buffer_load_dwordx4 v18, s[16:19], s55 offen nt lds\n"
    "ds_write_b128 v244, v[228:231] offset:0\n"
    "s_add_u32 m0, s45, 0x1cc00\n"
    "s_add_u32 s52, s48, 0x36000\n"
    "buffer_load_dwordx4 v19, s[16:19], s52 offen nt lds\n"
    "ds_write_b64 v245, v[232:233] offset:0\n"
    "s_waitcnt lgkmcnt(4)\n"
    "v_mfma_f32_16x16x32_bf16 v[148:151], a[220:223], v[112:115], v[148:151]\n"
    "s_add_u32 m0, s45, 0x1dc00\n"
    "s_add_u32 s53, s48, 0x36000\n"
    "buffer_load_dwordx4 v20, s[16:19], s53 offen nt lds\n"
    "v_mfma_f32_16x16x32_bf16 v[152:155], a[224:227], v[112:115], v[152:155]\n"
    "ds_write_b128 v246, v[234:237] offset:0\n"
    "v_mfma_f32_16x16x32_bf16 v[156:159], a[228:231], v[112:115], v[156:159]\n"
    "s_waitcnt lgkmcnt(3)\n"
    "ds_read_b128 v[112:115], v7 offset:18432\n"
    "v_mfma_f32_16x16x32_bf16 v[148:151], a[232:235], v[116:119], v[148:151]\n"
    "s_add_u32 m0, s45, 0x1ec00\n"
    "s_add_u32 s54, s48, 0x36000\n"
    "buffer_load_dwordx4 v21, s[16:19], s54 offen nt lds\n"
    "v_mfma_f32_16x16x32_bf16 v[152:155], a[236:239], v[116:119], v[152:155]\n"
    "ds_write_b64 v247, v[238:239] offset:0\n"
    "v_mfma_f32_16x16x32_bf16 v[156:159], a[240:243], v[116:119], v[156:159]\n"
    "s_waitcnt vmcnt(11) lgkmcnt(4)\n"
    "v_lshlrev_b32 v184, 16, v190\n"
    "v_mfma_f32_16x16x32_bf16 v[148:151], a[244:247], v[120:123], v[148:151]\n"
    "ds_read_b128 v[116:119], v7 offset:18496\n"
    "v_and_b32 v185, 0xffff0000, v190\n"
    "v_mfma_f32_16x16x32_bf16 v[152:155], a[248:251], v[120:123], v[152:155]\n"
    "v_lshlrev_b32 v186, 16, v191\n"
    "v_and_b32 v187, 0xffff0000, v191\n"
    "v_mfma_f32_16x16x32_bf16 v[156:159], a[252:255], v[120:123], v[156:159]\n"
    "ds_read_b128 v[120:123], v7 offset:18560\n"
    "v_lshlrev_b32 v188, 16, v192\n"
    "v_sub_f32 v220, v124, v148\n"
    "v_sub_f32 v221, v125, v149\n"
    "v_cvt_pk_bf16_f32 v228, v220, v221\n"
    "v_sub_f32 v222, v126, v150\n"
    "v_sub_f32 v223, v127, v151\n"
    "v_cvt_pk_bf16_f32 v229, v222, v223\n"
    "v_sub_f32 v224, v128, v152\n"
    "v_sub_f32 v225, v129, v153\n"
    "v_cvt_pk_bf16_f32 v230, v224, v225\n"
    "v_sub_f32 v226, v130, v154\n"
    "v_sub_f32 v227, v131, v155\n"
    "v_cvt_pk_bf16_f32 v231, v226, v227\n"
    "ds_write_b128 v8, v[228:231] offset:0\n"
    "v_sub_f32 v220, v132, v156\n"
    "v_sub_f32 v221, v133, v157\n"
    "v_cvt_pk_bf16_f32 v232, v220, v221\n"
    "s_add_u32 m0, s46, 0x1fc00\n"
    "s_add_u32 s55, s48, 0x36000\n"
    "s_mov_b32 exec_hi, 0\n"
    "buffer_load_dwordx4 v22, s[16:19], s55 offen nt lds\n"
    "s_mov_b32 exec_hi, -1\n"
    "v_sub_f32 v222, v134, v158\n"
    "v_sub_f32 v223, v135, v159\n"
    "v_cvt_pk_bf16_f32 v233, v222, v223\n"
    "ds_write_b64 v9, v[232:233] offset:0\n"
    "v_and_b32 v189, 0xffff0000, v192\n"
    "v_lshlrev_b32 v190, 16, v193\n"
    "v_and_b32 v191, 0xffff0000, v193\n"
    "s_waitcnt vmcnt(11) lgkmcnt(3)\n"
    "v_lshlrev_b32 v192, 16, v194\n"
    "v_and_b32 v193, 0xffff0000, v194\n"
    "v_lshlrev_b32 v194, 16, v195\n"
    "v_and_b32 v195, 0xffff0000, v195\n"
    "v_add_f32 v224, v136, v148\n"
    "v_add_f32 v225, v137, v149\n"
    "v_cvt_pk_bf16_f32 v234, v224, v225\n"
    "v_add_f32 v226, v138, v150\n"
    "v_add_f32 v227, v139, v151\n"
    "v_cvt_pk_bf16_f32 v235, v226, v227\n"
    "v_add_f32 v220, v140, v152\n"
    "v_add_f32 v221, v141, v153\n"
    "v_cvt_pk_bf16_f32 v236, v220, v221\n"
    "v_add_f32 v222, v142, v154\n"
    "v_add_f32 v223, v143, v155\n"
    "v_cvt_pk_bf16_f32 v237, v222, v223\n"
    "ds_write_b128 v8, v[234:237] offset:9600\n"
    "v_add_f32 v224, v144, v156\n"
    "v_add_f32 v225, v145, v157\n"
    "v_cvt_pk_bf16_f32 v238, v224, v225\n"
    "v_add_f32 v226, v146, v158\n"
    "v_add_f32 v227, v147, v159\n"
    "v_cvt_pk_bf16_f32 v239, v226, v227\n"
    "ds_write_b64 v9, v[238:239] offset:9600\n"
    "v_sub_f32 v220, v148, v136\n"
    "s_waitcnt vmcnt(10) lgkmcnt(3)\n"
    "v_lshlrev_b32 v160, 16, v208\n"
    "v_and_b32 v161, 0xffff0000, v208\n"
    "v_lshlrev_b32 v162, 16, v209\n"
    "v_and_b32 v163, 0xffff0000, v209\n"
    "v_lshlrev_b32 v164, 16, v210\n"
    "v_and_b32 v165, 0xffff0000, v210\n"
    "v_mfma_f32_16x16x32_bf16 v[160:163], a[220:223], v[100:103], v[160:163]\n"
    "s_add_u32 s52, s49, 0x12000\n"
    "buffer_load_dwordx4 v[202:205], v23, s[20:23], s52 offen offset:0 \n"
    "v_lshlrev_b32 v166, 16, v211\n"
    "v_and_b32 v167, 0xffff0000, v211\n"
    "v_sub_f32 v221, v149, v137\n"
    "v_cvt_pk_bf16_f32 v228, v220, v221\n"
    "v_mfma_f32_16x16x32_bf16 v[164:167], a[224:227], v[100:103], v[164:167]\n"
    "v_sub_f32 v222, v150, v138\n"
    "v_sub_f32 v223, v151, v139\n"
    "v_cvt_pk_bf16_f32 v229, v222, v223\n"
    "v_sub_f32 v224, v152, v140\n"
    "v_sub_f32 v225, v153, v141\n"
    "v_cvt_pk_bf16_f32 v230, v224, v225\n"
    "v_sub_f32 v226, v154, v142\n"
    "v_sub_f32 v227, v155, v143\n"
    "v_cvt_pk_bf16_f32 v231, v226, v227\n"
    "ds_write_b128 v8, v[228:231] offset:19200\n"
    "v_sub_f32 v220, v156, v144\n"
    "v_sub_f32 v221, v157, v145\n"
    "v_cvt_pk_bf16_f32 v232, v220, v221\n"
    "v_sub_f32 v222, v158, v146\n"
    "v_sub_f32 v223, v159, v147\n"
    "v_cvt_pk_bf16_f32 v233, v222, v223\n"
    "ds_write_b64 v9, v[232:233] offset:19200\n"
    "s_waitcnt vmcnt(10) lgkmcnt(1)\n"
    "v_lshlrev_b32 v168, 16, v212\n"
    "v_and_b32 v169, 0xffff0000, v212\n"
    "v_lshlrev_b32 v170, 16, v213\n"
    "v_and_b32 v171, 0xffff0000, v213\n"
    "s_add_u32 s53, s49, 0x12000\n"
    "buffer_load_dwordx2 v[206:207], v24, s[20:23], s53 offen offset:64 \n"
    "v_mfma_f32_16x16x32_bf16 v[168:171], a[228:231], v[100:103], v[168:171]\n"
    "ds_read_b128 v[100:103], v6 offset:27648\n"
    "v_mfma_f32_16x16x32_bf16 v[160:163], a[232:235], v[104:107], v[160:163]\n"
    "v_mfma_f32_16x16x32_bf16 v[164:167], a[236:239], v[104:107], v[164:167]\n"
    "v_mfma_f32_16x16x32_bf16 v[168:171], a[240:243], v[104:107], v[168:171]\n"
    "ds_read_b128 v[104:107], v6 offset:27712\n"
    "v_mfma_f32_16x16x32_bf16 v[160:163], a[244:247], v[108:111], v[160:163]\n"
    "v_mfma_f32_16x16x32_bf16 v[164:167], a[248:251], v[108:111], v[164:167]\n"
    "v_mfma_f32_16x16x32_bf16 v[168:171], a[252:255], v[108:111], v[168:171]\n"
    "ds_read_b128 v[108:111], v6 offset:27776\n"
    "s_waitcnt lgkmcnt(2)\n"
    "v_mfma_f32_16x16x32_bf16 v[124:127], a[220:223], v[100:103], v[184:187]\n"
    "v_mfma_f32_16x16x32_bf16 v[128:131], a[224:227], v[100:103], v[188:191]\n"
    "s_add_u32 s54, s49, 0x9000\n"
    "buffer_load_dwordx4 v[208:211], v25, s[20:23], s54 offen offset:0 \n"
    "v_mfma_f32_16x16x32_bf16 v[132:135], a[228:231], v[100:103], v[192:195]\n"
    "s_waitcnt lgkmcnt(1)\n"
    "s_waitcnt vmcnt(11)\n"
    "v_lshlrev_b32 v172, 16, v214\n"
    "v_mfma_f32_16x16x32_bf16 v[124:127], a[232:235], v[104:107], v[124:127]\n"
    "v_and_b32 v173, 0xffff0000, v214\n"
    "v_lshlrev_b32 v174, 16, v215\n"
    "v_mfma_f32_16x16x32_bf16 v[128:131], a[236:239], v[104:107], v[128:131]\n"
    "v_and_b32 v175, 0xffff0000, v215\n"
    "v_lshlrev_b32 v176, 16, v216\n"
    "v_mfma_f32_16x16x32_bf16 v[132:135], a[240:243], v[104:107], v[132:135]\n"
    "s_waitcnt lgkmcnt(0)\n"
    "v_and_b32 v177, 0xffff0000, v216\n"
    "v_lshlrev_b32 v178, 16, v217\n"
    "v_mfma_f32_16x16x32_bf16 v[124:127], a[244:247], v[108:111], v[124:127]\n"
    "s_barrier\n"
    "v_and_b32 v179, 0xffff0000, v217\n"
    "v_mfma_f32_16x16x32_bf16 v[128:131], a[248:251], v[108:111], v[128:131]\n"
    "v_mfma_f32_16x16x32_bf16 v[132:135], a[252:255], v[108:111], v[132:135]\n"
    "v_mfma_f32_16x16x32_bf16 v[172:175], a[220:223], v[112:115], v[172:175]\n"
    "s_nop 2\n"
    "v_sub_f32 v224, v136, v124\n"
    "v_sub_f32 v225, v137, v125\n"
    "v_mfma_f32_16x16x32_bf16 v[176:179], a[224:227], v[112:115], v[176:179]\n"
    "v_cvt_pk_bf16_f32 v234, v224, v225\n"
    "v_sub_f32 v226, v138, v126\n"
    "v_sub_f32 v227, v139, v127\n"
    "v_cvt_pk_bf16_f32 v235, v226, v227\n"
    "v_sub_f32 v220, v140, v128\n"
    "v_sub_f32 v221, v141, v129\n"
    "v_cvt_pk_bf16_f32 v236, v220, v221\n"
    "v_sub_f32 v222, v142, v130\n"
    "v_sub_f32 v223, v143, v131\n"
    "v_cvt_pk_bf16_f32 v237, v222, v223\n"
    "ds_write_b128 v8, v[234:237] offset:28800\n"
    "s_waitcnt vmcnt(10)\n"
    "v_lshlrev_b32 v180, 16, v218\n"
    "v_and_b32 v181, 0xffff0000, v218\n"
    "v_lshlrev_b32 v182, 16, v219\n"
    "v_and_b32 v183, 0xffff0000, v219\n"
    "s_add_u32 s55, s49, 0x9000\n"
    "buffer_load_dwordx2 v[212:213], v26, s[20:23], s55 offen offset:64 \n"
    "v_mfma_f32_16x16x32_bf16 v[180:183], a[228:231], v[112:115], v[180:183]\n"
    "v_sub_f32 v224, v144, v132\n"
    "v_sub_f32 v225, v145, v133\n"
    "v_mfma_f32_16x16x32_bf16 v[172:175], a[232:235], v[116:119], v[172:175]\n"
    "v_cvt_pk_bf16_f32 v238, v224, v225\n"
    "v_sub_f32 v226, v146, v134\n"
    "v_mfma_f32_16x16x32_bf16 v[176:179], a[236:239], v[116:119], v[176:179]\n"
    "v_sub_f32 v227, v147, v135\n"
    "v_cvt_pk_bf16_f32 v239, v226, v227\n"
    "v_mfma_f32_16x16x32_bf16 v[180:183], a[240:243], v[116:119], v[180:183]\n"
    "ds_write_b64 v9, v[238:239] offset:28800\n"
    "v_mfma_f32_16x16x32_bf16 v[172:175], a[244:247], v[120:123], v[172:175]\n"
    "v_mfma_f32_16x16x32_bf16 v[176:179], a[248:251], v[120:123], v[176:179]\n"
    "v_mfma_f32_16x16x32_bf16 v[180:183], a[252:255], v[120:123], v[180:183]\n"
    "s_nop 5\n"
    "v_sub_f32 v220, v160, v172\n"
    "v_sub_f32 v221, v161, v173\n"
    "v_cvt_pk_bf16_f32 v228, v220, v221\n"
    "v_sub_f32 v222, v162, v174\n"
    "v_sub_f32 v223, v163, v175\n"
    "v_cvt_pk_bf16_f32 v229, v222, v223\n"
    "v_sub_f32 v224, v164, v176\n"
    "v_sub_f32 v225, v165, v177\n"
    "v_cvt_pk_bf16_f32 v230, v224, v225\n"
    "s_waitcnt lgkmcnt(0)\n"
    "s_barrier\n"
    "ds_read_b128 v[76:79], v0 offset:0\n"
    "ds_read_b128 v[80:83], v0 offset:64\n"
    "ds_read_b128 v[84:87], v0 offset:128\n"
    "ds_read_b128 v[88:91], v1 offset:0\n"
    "ds_read_b128 v[92:95], v1 offset:64\n"
    "ds_read_b128 v[96:99], v1 offset:128\n"
    "v_sub_f32 v226, v166, v178\n"
    "v_sub_f32 v227, v167, v179\n"
    "v_cvt_pk_bf16_f32 v231, v226, v227\n"
    "ds_write_b128 v12, v[228:231] offset:0\n"
    "v_sub_f32 v220, v168, v180\n"
    "v_sub_f32 v221, v169, v181\n"
    "v_cvt_pk_bf16_f32 v232, v220, v221\n"
    "v_sub_f32 v222, v170, v182\n"
    "v_sub_f32 v223, v171, v183\n"
    "v_cvt_pk_bf16_f32 v233, v222, v223\n"
    "ds_write_b64 v13, v[232:233] offset:0\n"
    "v_add_f32_dpp v224, v160, v172 row_ror:8 row_mask:0xf bank_mask:0x3\n"
    "s_waitcnt lgkmcnt(7)\n"
    "v_sub_f32_dpp v224, v172, v160 row_ror:8 row_mask:0xf bank_mask:0xc\n"
    "v_add_f32_dpp v225, v161, v173 row_ror:8 row_mask:0xf bank_mask:0x3\n"
    "v_mfma_f32_16x16x32_bf16 v[32:35], a[6:9], v[76:79], 0\n"
    "ds_read_b128 v[240:243], v29 offset:0\n"
    "v_sub_f32_dpp v225, v173, v161 row_ror:8 row_mask:0xf bank_mask:0xc\n"
    "v_mfma_f32_16x16x32_bf16 v[36:39], a[14:17], v[76:79], 0\n"
    "v_cvt_pk_bf16_f32 v234, v224, v225\n"
    "v_add_f32_dpp v226, v162, v174 row_ror:8 row_mask:0xf bank_mask:0x3\n"
    "v_mfma_f32_16x16x32_bf16 v[40:43], a[22:25], v[76:79], 0\n"
    "s_waitcnt lgkmcnt(7)\n"
    "ds_read_b128 v[76:79], v2 offset:0\n"
    "v_mfma_f32_16x16x32_bf16 v[32:35], a[30:33], v[80:83], v[32:35]\n"
    "s_add_u32 m0, s45, 0x20400\n"
    "s_add_u32 s52, s48, 0x5a000\n"
    "buffer_load_dwordx4 v18, s[16:19], s52 offen nt lds\n"
    "v_mfma_f32_16x16x32_bf16 v[36:39], a[38:41], v[80:83], v[36:39]\n"
    "s_waitcnt lgkmcnt(4)\n"
    "v_sub_f32_dpp v226, v174, v162 row_ror:8 row_mask:0xf bank_mask:0xc\n"
    "v_add_f32_dpp v227, v163, v175 row_ror:8 row_mask:0xf bank_mask:0x3\n"
    "v_mfma_f32_16x16x32_bf16 v[40:43], a[46:49], v[80:83], v[40:43]\n"
    "ds_read_b128 v[80:83], v2 offset:64\n"
    "v_sub_f32_dpp v227, v175, v163 row_ror:8 row_mask:0xf bank_mask:0xc\n"
    "v_mfma_f32_16x16x32_bf16 v[32:35], a[54:57], v[84:87], v[32:35]\n"
    "v_cvt_pk_bf16_f32 v235, v226, v227\n"
    "v_add_f32_dpp v220, v164, v176 row_ror:8 row_mask:0xf bank_mask:0x3\n"
    "v_mfma_f32_16x16x32_bf16 v[36:39], a[62:65], v[84:87], v[36:39]\n"
    "v_sub_f32_dpp v220, v176, v164 row_ror:8 row_mask:0xf bank_mask:0xc\n"
    "v_add_f32_dpp v221, v165, v177 row_ror:8 row_mask:0xf bank_mask:0x3\n"
    "v_mfma_f32_16x16x32_bf16 v[40:43], a[70:73], v[84:87], v[40:43]\n"
    "ds_read_b128 v[84:87], v2 offset:128\n"
    "v_sub_f32_dpp v221, v177, v165 row_ror:8 row_mask:0xf bank_mask:0xc\n"
    "v_mfma_f32_16x16x32_bf16 v[32:35], a[78:81], v[88:91], v[32:35]\n"
    "v_cvt_pk_bf16_f32 v236, v220, v221\n"
    "v_add_f32_dpp v222, v166, v178 row_ror:8 row_mask:0xf bank_mask:0x3\n"
    "v_mfma_f32_16x16x32_bf16 v[36:39], a[86:89], v[88:91], v[36:39]\n"
    "v_sub_f32_dpp v222, v178, v166 row_ror:8 row_mask:0xf bank_mask:0xc\n"
    "v_add_f32_dpp v223, v167, v179 row_ror:8 row_mask:0xf bank_mask:0x3\n"
    "v_mfma_f32_16x16x32_bf16 v[40:43], a[94:97], v[88:91], v[40:43]\n"
    "ds_read_b128 v[88:91], v3 offset:0\n"
    "v_sub_f32_dpp v223, v179, v167 row_ror:8 row_mask:0xf bank_mask:0xc\n"
    "v_mfma_f32_16x16x32_bf16 v[32:35], a[102:105], v[92:95], v[32:35]\n"
    "s_waitcnt lgkmcnt(4)\n"
    "v_cvt_pk_bf16_f32 v237, v222, v223\n"
    "v_add_f32_dpp v224, v168, v180 row_ror:8 row_mask:0xf bank_mask:0x3\n"
    "v_mfma_f32_16x16x32_bf16 v[36:39], a[110:113], v[92:95], v[36:39]\n"
    "ds_write_b128 v14, v[234:237] offset:0\n"
    "v_mfma_f32_16x16x32_bf16 v[40:43], a[118:121], v[92:95], v[40:43]\n"
    "s_add_u32 m0, s45, 0x21400\n"
    "s_add_u32 s53, s48, 0x5a000\n"
    "buffer_load_dwordx4 v19, s[16:19], s53 offen nt lds\n"
    "v_mfma_f32_16x16x32_bf16 v[32:35], a[126:129], v[96:99], v[32:35]\n"
    "ds_read_b128 v[92:95], v3 offset:64\n"
    "v_sub_f32_dpp v224, v180, v168 row_ror:8 row_mask:0xf bank_mask:0xc\n"
    "v_mfma_f32_16x16x32_bf16 v[36:39], a[134:137], v[96:99], v[36:39]\n"
    "s_waitcnt lgkmcnt(4)\n"
    "v_add_f32_dpp v225, v169, v181 row_ror:8 row_mask:0xf bank_mask:0x3\n"
    "v_sub_f32_dpp v225, v181, v169 row_ror:8 row_mask:0xf bank_mask:0xc\n"
    "v_mfma_f32_16x16x32_bf16 v[40:43], a[142:145], v[96:99], v[40:43]\n"
    "ds_read_b128 v[96:99], v3 offset:128\n"
    "v_cvt_pk_bf16_f32 v238, v224, v225\n"
    "v_mfma_f32_16x16x32_bf16 v[32:35], a[150:153], v[76:79], v[32:35]\n"
    "s_waitcnt lgkmcnt(4)\n"
    "v_add_f32_dpp v226, v170, v182 row_ror:8 row_mask:0xf bank_mask:0x3\n"
    "v_sub_f32_dpp v226, v182, v170 row_ror:8 row_mask:0xf bank_mask:0xc\n"
    "v_mfma_f32_16x16x32_bf16 v[36:39], a[158:161], v[76:79], v[36:39]\n"
    "v_add_f32_dpp v227, v171, v183 row_ror:8 row_mask:0xf bank_mask:0x3\n"
    "v_sub_f32_dpp v227, v183, v171 row_ror:8 row_mask:0xf bank_mask:0xc\n"
    "v_mfma_f32_16x16x32_bf16 v[40:43], a[166:169], v[76:79], v[40:43]\n"
    "ds_read_b128 v[76:79], v4 offset:0\n"
    "v_cvt_pk_bf16_f32 v239, v226, v227\n"
    "v_mfma_f32_16x16x32_bf16 v[32:35], a[174:177], v[80:83], v[32:35]\n"
    "ds_write_b64 v15, v[238:239] offset:0\n"
    "v_mfma_f32_16x16x32_bf16 v[36:39], a[182:185], v[80:83], v[36:39]\n"
    "s_waitcnt lgkmcnt(5)\n"
    "v_mfma_f32_16x16x32_bf16 v[40:43], a[190:193], v[80:83], v[40:43]\n"
    "ds_read_b128 v[80:83], v4 offset:64\n"
    "v_mfma_f32_16x16x32_bf16 v[32:35], a[198:201], v[84:87], v[32:35]\n"
    "v_mfma_f32_16x16x32_bf16 v[36:39], a[206:209], v[84:87], v[36:39]\n"
    "v_mfma_f32_16x16x32_bf16 v[40:43], a[214:217], v[84:87], v[40:43]\n"
    "ds_read_b128 v[84:87], v4 offset:128\n"
    "s_waitcnt lgkmcnt(5)\n"
    "v_mfma_f32_16x16x32_bf16 v[44:47], a[2:5], v[88:91], v[32:35]\n"
    "s_nop 2\n"
    "ds_write_b128 v16, v[32:35] offset:0\n"
    "v_mfma_f32_16x16x32_bf16 v[48:51], a[10:13], v[88:91], v[36:39]\n"
    "s_waitcnt lgkmcnt(5)\n"
    "v_mfma_f32_16x16x32_bf16 v[52:55], a[18:21], v[88:91], v[40:43]\n"
    "ds_read_b128 v[88:91], v5 offset:0\n"
    "v_mfma_f32_16x16x32_bf16 v[44:47], a[26:29], v[92:95], v[44:47]\n"
    "ds_write_b128 v16, v[36:39] offset:1024\n"
    "v_mfma_f32_16x16x32_bf16 v[48:51], a[34:37], v[92:95], v[48:51]\n"
    "s_waitcnt lgkmcnt(6)\n"
    "v_mfma_f32_16x16x32_bf16 v[52:55], a[42:45], v[92:95], v[52:55]\n"
    "ds_read_b128 v[92:95], v5 offset:64\n"
    "s_waitcnt lgkmcnt(5)\n"
    "v_mfma_f32_16x16x32_bf16 v[44:47], a[50:53], v[96:99], v[44:47]\n"
    "ds_write_b128 v16, v[40:43] offset:2048\n"
    "v_mfma_f32_16x16x32_bf16 v[48:51], a[58:61], v[96:99], v[48:51]\n"
    "s_add_u32 m0, s45, 0x22400\n"
    "s_add_u32 s54, s48, 0x5a000\n"
    "buffer_load_dwordx4 v20, s[16:19], s54 offen nt lds\n"
    "v_mfma_f32_16x16x32_bf16 v[52:55], a[66:69], v[96:99], v[52:55]\n"
    "ds_read_b128 v[96:99], v5 offset:128\n"
    "s_waitcnt lgkmcnt(6)\n"
    "v_mfma_f32_16x16x32_bf16 v[44:47], a[74:77], v[76:79], v[44:47]\n"
    "v_mfma_f32_16x16x32_bf16 v[48:51], a[82:85], v[76:79], v[48:51]\n"
    "v_mfma_f32_16x16x32_bf16 v[52:55], a[90:93], v[76:79], v[52:55]\n"
    "ds_read_b128 v[76:79], v0 offset:3072\n"
    "s_waitcnt lgkmcnt(5)\n"
    "v_mfma_f32_16x16x32_bf16 v[44:47], a[98:101], v[80:83], v[44:47]\n"
    "v_mfma_f32_16x16x32_bf16 v[48:51], a[106:109], v[80:83], v[48:51]\n"
    "v_mfma_f32_16x16x32_bf16 v[52:55], a[114:117], v[80:83], v[52:55]\n"
    "ds_read_b128 v[80:83], v0 offset:3136\n"
    "v_mfma_f32_16x16x32_bf16 v[44:47], a[122:125], v[84:87], v[44:47]\n"
    "s_waitcnt lgkmcnt(4)\n"
    "v_mfma_f32_16x16x32_bf16 v[48:51], a[130:133], v[84:87], v[48:51]\n"
    "v_mfma_f32_16x16x32_bf16 v[52:55], a[138:141], v[84:87], v[52:55]\n"
    "ds_read_b128 v[84:87], v0 offset:3200\n"
    "v_mfma_f32_16x16x32_bf16 v[44:47], a[146:149], v[88:91], v[44:47]\n"
    "s_add_u32 m0, s45, 0x23400\n"
    "s_add_u32 s55, s48, 0x5a000\n"
    "buffer_load_dwordx4 v21, s[16:19], s55 offen nt lds\n"
    "v_mfma_f32_16x16x32_bf16 v[48:51], a[154:157], v[88:91], v[48:51]\n"
    "s_waitcnt lgkmcnt(3)\n"
    "v_mfma_f32_16x16x32_bf16 v[52:55], a[162:165], v[88:91], v[52:55]\n"
    "ds_read_b128 v[88:91], v1 offset:3072\n"
    "v_mfma_f32_16x16x32_bf16 v[44:47], a[170:173], v[92:95], v[44:47]\n"
    "s_add_u32 m0, s46, 0x24400\n"
    "s_add_u32 s52, s48, 0x5a000\n"
    "s_mov_b32 exec_hi, 0\n"
    "buffer_load_dwordx4 v22, s[16:19], s52 offen nt lds\n"
    "s_mov_b32 exec_hi, -1\n"
    "v_mfma_f32_16x16x32_bf16 v[48:51], a[178:181], v[92:95], v[48:51]\n"
    "s_waitcnt lgkmcnt(3)\n"
    "v_mfma_f32_16x16x32_bf16 v[52:55], a[186:189], v[92:95], v[52:55]\n"
    "ds_read_b128 v[92:95], v1 offset:3136\n"
    "v_mfma_f32_16x16x32_bf16 v[44:47], a[194:197], v[96:99], v[44:47]\n"
    "s_waitcnt lgkmcnt(3)\n"
    "v_mfma_f32_16x16x32_bf16 v[48:51], a[202:205], v[96:99], v[48:51]\n"
    "v_mfma_f32_16x16x32_bf16 v[52:55], a[210:213], v[96:99], v[52:55]\n"
    "ds_read_b128 v[96:99], v1 offset:3200\n"
    "s_add_u32 s55, s49, 0x12000\n"
    "buffer_load_dwordx4 v[214:217], v25, s[20:23], s55 offen offset:0 \n"
    "s_add_u32 s52, s49, 0x12000\n"
    "buffer_load_dwordx2 v[218:219], v26, s[20:23], s52 offen offset:64 \n"
    "s_waitcnt vmcnt(11) lgkmcnt(0)\n"
    "s_barrier\n"
    "ds_read_b128 v[100:103], v6 offset:0\n"
    "ds_read_b128 v[104:107], v6 offset:64\n"
    "ds_read_b128 v[108:111], v6 offset:128\n"
    "ds_read_b128 v[112:115], v6 offset:9216\n"
    "ds_read_b128 v[116:119], v6 offset:9280\n"
    "ds_read_b128 v[120:123], v6 offset:9344\n"
    "ds_read_b128 v[56:59], v17 offset:0\n"
    "ds_read_b128 v[60:63], v17 offset:1024\n"
    "ds_read_b128 v[64:67], v17 offset:2048\n"
    "ds_write_b128 v30, v[240:243] offset:38400\n"
    "s_waitcnt lgkmcnt(9)\n"
    "v_mfma_f32_16x16x32_bf16 v[136:139], a[220:223], v[100:103], v[184:187]\n"
    "v_mfma_f32_16x16x32_bf16 v[140:143], a[224:227], v[100:103], v[188:191]\n"
    "v_mfma_f32_16x16x32_bf16 v[144:147], a[228:231], v[100:103], v[192:195]\n"
    "s_waitcnt lgkmcnt(8)\n"
    "ds_read_b128 v[100:103], v7 offset:0\n"
    "v_mfma_f32_16x16x32_bf16 v[136:139], a[232:235], v[104:107], v[136:139]\n"
    "s_waitcnt lgkmcnt(7)\n"
    "s_waitcnt lgkmcnt(4)\n"
    "v_fma_f32 v56, v44, s44, v56\n"
    "v_mfma_f32_16x16x32_bf16 v[140:143], a[236:239], v[104:107], v[140:143]\n"
    "v_fma_f32 v57, v45, s44, v57\n"
    "v_cvt_pk_bf16_f32 v68, v56, v57\n"
    "v_mfma_f32_16x16x32_bf16 v[144:147], a[240:243], v[104:107], v[144:147]\n"
    "ds_read_b128 v[104:107], v7 offset:64\n"
    "v_fma_f32 v58, v46, s44, v58\n"
    "v_mfma_f32_16x16x32_bf16 v[136:139], a[244:247], v[108:111], v[136:139]\n"
    "s_waitcnt lgkmcnt(3)\n"
    "v_fma_f32 v59, v47, s44, v59\n"
    "v_cvt_pk_bf16_f32 v69, v58, v59\n"
    "v_mfma_f32_16x16x32_bf16 v[140:143], a[248:251], v[108:111], v[140:143]\n"
    "v_fma_f32 v60, v48, s44, v60\n"
    "v_fma_f32 v61, v49, s44, v61\n"
    "v_mfma_f32_16x16x32_bf16 v[144:147], a[252:255], v[108:111], v[144:147]\n"
    "ds_read_b128 v[108:111], v7 offset:128\n"
    "v_cvt_pk_bf16_f32 v70, v60, v61\n"
    "v_mfma_f32_16x16x32_bf16 v[32:35], a[6:9], v[76:79], 0\n"
    "v_fma_f32 v62, v50, s44, v62\n"
    "v_fma_f32 v63, v51, s44, v63\n"
    "v_mfma_f32_16x16x32_bf16 v[36:39], a[14:17], v[76:79], 0\n"
    "v_cvt_pk_bf16_f32 v71, v62, v63\n"
    "v_fma_f32 v64, v52, s44, v64\n"
    "v_mfma_f32_16x16x32_bf16 v[40:43], a[22:25], v[76:79], 0\n"
    "ds_read_b128 v[76:79], v2 offset:3072\n"
    "v_fma_f32 v65, v53, s44, v65\n"
    "v_mfma_f32_16x16x32_bf16 v[32:35], a[30:33], v[80:83], v[32:35]\n"
    "ds_write_b128 v27, v[68:71] offset:0\n"
    "v_mfma_f32_16x16x32_bf16 v[36:39], a[38:41], v[80:83], v[36:39]\n"
    "s_waitcnt lgkmcnt(4)\n"
    "v_cvt_pk_bf16_f32 v72, v64, v65\n"
    "v_fma_f32 v66, v54, s44, v66\n"
    "v_mfma_f32_16x16x32_bf16 v[40:43], a[46:49], v[80:83], v[40:43]\n"
    "ds_read_b128 v[80:83], v2 offset:3136\n"
    "v_fma_f32 v67, v55, s44, v67\n"
    "v_mfma_f32_16x16x32_bf16 v[32:35], a[54:57], v[84:87], v[32:35]\n"
    "s_waitcnt lgkmcnt(4)\n"
    "v_cvt_pk_bf16_f32 v73, v66, v67\n"
    "s_waitcnt vmcnt(10)\n"
    "v_lshlrev_b32 v184, 16, v202\n"
    "v_mfma_f32_16x16x32_bf16 v[36:39], a[62:65], v[84:87], v[36:39]\n"
    "ds_write_b64 v28, v[72:73] offset:0\n"
    "v_mfma_f32_16x16x32_bf16 v[40:43], a[70:73], v[84:87], v[40:43]\n"
    "v_and_b32 v185, 0xffff0000, v202\n"
    "v_lshlrev_b32 v186, 16, v203\n"
    "v_mfma_f32_16x16x32_bf16 v[32:35], a[78:81], v[88:91], v[32:35]\n"
    "ds_read_b128 v[84:87], v2 offset:3200\n"
    "v_and_b32 v187, 0xffff0000, v203\n"
    "v_mfma_f32_16x16x32_bf16 v[36:39], a[86:89], v[88:91], v[36:39]\n"
    "s_waitcnt lgkmcnt(5)\n"
    "v_lshlrev_b32 v188, 16, v204\n"
    "v_and_b32 v189, 0xffff0000, v204\n"
    "v_mfma_f32_16x16x32_bf16 v[40:43], a[94:97], v[88:91], v[40:43]\n"
    "ds_read_b128 v[88:91], v3 offset:3072\n"
    "v_lshlrev_b32 v190, 16, v205\n"
    "v_mfma_f32_16x16x32_bf16 v[32:35], a[102:105], v[92:95], v[32:35]\n"
    "s_waitcnt lgkmcnt(5)\n"
    "v_and_b32 v191, 0xffff0000, v205\n"
    "s_waitcnt vmcnt(9) lgkmcnt(4)\n"
    "v_lshlrev_b32 v192, 16, v206\n"
    "v_mfma_f32_16x16x32_bf16 v[36:39], a[110:113], v[92:95], v[36:39]\n"
    "s_add_u32 s54, s49, 0x24000\n"
    "buffer_load_dwordx4 v[202:205], v23, s[20:23], s54 offen offset:0 \n"
    "v_mfma_f32_16x16x32_bf16 v[40:43], a[118:121], v[92:95], v[40:43]\n"
    "ds_read_b128 v[92:95], v3 offset:3136\n"
    "v_and_b32 v193, 0xffff0000, v206\n"
    "v_mfma_f32_16x16x32_bf16 v[32:35], a[126:129], v[96:99], v[32:35]\n"
    "s_waitcnt lgkmcnt(4)\n"
    "v_lshlrev_b32 v194, 16, v207\n"
    "v_and_b32 v195, 0xffff0000, v207\n"
    "v_mfma_f32_16x16x32_bf16 v[36:39], a[134:137], v[96:99], v[36:39]\n"
    "v_sub_f32 v220, v148, v136\n"
    "v_sub_f32 v221, v149, v137\n"
    "v_mfma_f32_16x16x32_bf16 v[40:43], a[142:145], v[96:99], v[40:43]\n"
    "ds_read_b128 v[96:99], v3 offset:3200\n"
    "v_cvt_pk_bf16_f32 v228, v220, v221\n"
    "v_mfma_f32_16x16x32_bf16 v[32:35], a[150:153], v[76:79], v[32:35]\n"
    "s_add_u32 s55, s49, 0x24000\n"
    "buffer_load_dwordx2 v[206:207], v24, s[20:23], s55 offen offset:64 \n"
    "v_mfma_f32_16x16x32_bf16 v[36:39], a[158:161], v[76:79], v[36:39]\n"
    "v_sub_f32 v222, v150, v138\n"
    "v_sub_f32 v223, v151, v139\n"
    "v_mfma_f32_16x16x32_bf16 v[40:43], a[166:169], v[76:79], v[40:43]\n"
    "ds_read_b128 v[76:79], v4 offset:3072\n"
    "v_cvt_pk_bf16_f32 v229, v222, v223\n"
    "v_mfma_f32_16x16x32_bf16 v[32:35], a[174:177], v[80:83], v[32:35]\n"
    "s_waitcnt lgkmcnt(4)\n"
    "v_sub_f32 v224, v152, v140\n"
    "v_sub_f32 v225, v153, v141\n"
    "v_mfma_f32_16x16x32_bf16 v[36:39], a[182:185], v[80:83], v[36:39]\n"
    "s_waitcnt lgkmcnt(3)\n"
    "v_cvt_pk_bf16_f32 v230, v224, v225\n"
    "v_sub_f32 v226, v154, v142\n"
    "v_mfma_f32_16x16x32_bf16 v[40:43], a[190:193], v[80:83], v[40:43]\n"
    "ds_read_b128 v[80:83], v4 offset:3136\n"
    "v_sub_f32 v227, v155, v143\n"
    "v_mfma_f32_16x16x32_bf16 v[32:35], a[198:201], v[84:87], v[32:35]\n"
    "v_cvt_pk_bf16_f32 v231, v226, v227\n"
    "v_sub_f32 v220, v156, v144\n"
    "v_mfma_f32_16x16x32_bf16 v[36:39], a[206:209], v[84:87], v[36:39]\n"
    "ds_write_b128 v10, v[228:231] offset:0\n"
    "v_mfma_f32_16x16x32_bf16 v[40:43], a[214:217], v[84:87], v[40:43]\n"
    "s_waitcnt lgkmcnt(4)\n"
    "ds_read_b128 v[84:87], v4 offset:3200\n"
    "v_mfma_f32_16x16x32_bf16 v[148:151], a[220:223], v[112:115], v[184:187]\n"
    "s_waitcnt lgkmcnt(4)\n"
    "v_sub_f32 v221, v157, v145\n"
    "v_cvt_pk_bf16_f32 v232, v220, v221\n"
    "v_mfma_f32_16x16x32_bf16 v[152:155], a[224:227], v[112:115], v[188:191]\n"
    "ds_write_b128 v16, v[32:35] offset:12288\n"
    "v_sub_f32 v222, v158, v146\n"
    "v_sub_f32 v223, v159, v147\n"
    "v_mfma_f32_16x16x32_bf16 v[156:159], a[228:231], v[112:115], v[192:195]\n"
    "s_waitcnt lgkmcnt(4)\n"
    "v_cvt_pk_bf16_f32 v233, v222, v223\n"
    "v_add_f32 v224, v124, v136\n"
    "v_mfma_f32_16x16x32_bf16 v[148:151], a[232:235], v[116:119], v[148:151]\n"
    "ds_write_b64 v11, v[232:233] offset:0\n"
    "v_mfma_f32_16x16x32_bf16 v[152:155], a[236:239], v[116:119], v[152:155]\n"
    "v_add_f32 v225, v125, v137\n"
    "v_cvt_pk_bf16_f32 v234, v224, v225\n"
    "v_mfma_f32_16x16x32_bf16 v[156:159], a[240:243], v[116:119], v[156:159]\n"
    "s_waitcnt lgkmcnt(4)\n"
    "v_add_f32 v226, v126, v138\n"
    "v_add_f32 v227, v127, v139\n"
    "v_mfma_f32_16x16x32_bf16 v[148:151], a[244:247], v[120:123], v[148:151]\n"
    "ds_write_b128 v16, v[36:39] offset:13312\n"
    "v_mfma_f32_16x16x32_bf16 v[152:155], a[248:251], v[120:123], v[152:155]\n"
    "v_cvt_pk_bf16_f32 v235, v226, v227\n"
    "v_add_f32 v220, v128, v140\n"
    "v_mfma_f32_16x16x32_bf16 v[156:159], a[252:255], v[120:123], v[156:159]\n"
    "s_waitcnt lgkmcnt(3)\n"
    "v_add_f32 v221, v129, v141\n"
    "v_cvt_pk_bf16_f32 v236, v220, v221\n"
    "v_mfma_f32_16x16x32_bf16 v[44:47], a[2:5], v[88:91], v[32:35]\n"
    "ds_write_b128 v16, v[40:43] offset:14336\n"
    "v_mfma_f32_16x16x32_bf16 v[48:51], a[10:13], v[88:91], v[36:39]\n"
    "v_add_f32 v222, v130, v142\n"
    "v_add_f32 v223, v131, v143\n"
    "v_mfma_f32_16x16x32_bf16 v[52:55], a[18:21], v[88:91], v[40:43]\n"
    "ds_read_b128 v[88:91], v5 offset:3072\n"
    "v_cvt_pk_bf16_f32 v237, v222, v223\n"
    "v_mfma_f32_16x16x32_bf16 v[44:47], a[26:29], v[92:95], v[44:47]\n"
    "ds_write_b128 v10, v[234:237] offset:9600\n"
    "v_mfma_f32_16x16x32_bf16 v[48:51], a[34:37], v[92:95], v[48:51]\n"
    "v_add_f32 v224, v132, v144\n"
    "v_add_f32 v225, v133, v145\n"
    "v_mfma_f32_16x16x32_bf16 v[52:55], a[42:45], v[92:95], v[52:55]\n"
    "ds_read_b128 v[92:95], v5 offset:3136\n"
    "v_cvt_pk_bf16_f32 v238, v224, v225\n"
    "v_mfma_f32_16x16x32_bf16 v[44:47], a[50:53], v[96:99], v[44:47]\n"
    "s_waitcnt lgkmcnt(3)\n"
    "s_barrier\n"
    "v_add_f32 v226, v134, v146\n"
    "v_mfma_f32_16x16x32_bf16 v[48:51], a[58:61], v[96:99], v[48:51]\n"
    "ds_read_b128 v[56:59], v17 offset:12288\n"
    "v_add_f32 v227, v135, v147\n"
    "v_mfma_f32_16x16x32_bf16 v[52:55], a[66:69], v[96:99], v[52:55]\n"
    "ds_read_b128 v[96:99], v5 offset:3200\n"
    "v_cvt_pk_bf16_f32 v239, v226, v227\n"
    "v_mfma_f32_16x16x32_bf16 v[44:47], a[74:77], v[76:79], v[44:47]\n"
    "ds_write_b64 v11, v[238:239] offset:9600\n"
    "v_mfma_f32_16x16x32_bf16 v[48:51], a[82:85], v[76:79], v[48:51]\n"
    "v_sub_f32 v220, v136, v124\n"
    "v_sub_f32 v221, v137, v125\n"
    "v_mfma_f32_16x16x32_bf16 v[52:55], a[90:93], v[76:79], v[52:55]\n"
    "ds_read_b128 v[76:79], v0 offset:6144\n"
    "v_cvt_pk_bf16_f32 v228, v220, v221\n"
    "v_mfma_f32_16x16x32_bf16 v[44:47], a[98:101], v[80:83], v[44:47]\n"
    "ds_read_b128 v[60:63], v17 offset:13312\n"
    "v_sub_f32 v222, v138, v126\n"
    "v_mfma_f32_16x16x32_bf16 v[48:51], a[106:109], v[80:83], v[48:51]\n"
    "ds_read_b128 v[64:67], v17 offset:14336\n"
    "v_sub_f32 v223, v139, v127\n"
    "v_mfma_f32_16x16x32_bf16 v[52:55], a[114:117], v[80:83], v[52:55]\n"
    "ds_read_b128 v[80:83], v0 offset:6208\n"
    "v_cvt_pk_bf16_f32 v229, v222, v223\n"
    "v_mfma_f32_16x16x32_bf16 v[44:47], a[122:125], v[84:87], v[44:47]\n"
    "ds_read_b128 v[252:255], v248 offset:0\n"
    "v_sub_f32 v224, v140, v128\n"
    "v_mfma_f32_16x16x32_bf16 v[48:51], a[130:133], v[84:87], v[48:51]\n"
    "ds_read_b128 v[240:243], v249 offset:0\n"
    "v_sub_f32 v225, v141, v129\n"
    "v_mfma_f32_16x16x32_bf16 v[52:55], a[138:141], v[84:87], v[52:55]\n"
    "s_waitcnt lgkmcnt(9)\n"
    "ds_read_b128 v[84:87], v0 offset:6272\n"
    "v_mfma_f32_16x16x32_bf16 v[44:47], a[146:149], v[88:91], v[44:47]\n"
    "s_waitcnt lgkmcnt(9)\n"
    "v_cvt_pk_bf16_f32 v230, v224, v225\n"
    "v_sub_f32 v226, v142, v130\n"
    "v_mfma_f32_16x16x32_bf16 v[48:51], a[154:157], v[88:91], v[48:51]\n"
    "s_waitcnt lgkmcnt(8)\n"
    "v_sub_f32 v227, v143, v131\n"
    "v_cvt_pk_bf16_f32 v231, v226, v227\n"
    "v_mfma_f32_16x16x32_bf16 v[52:55], a[162:165], v[88:91], v[52:55]\n"
    "ds_read_b128 v[88:91], v1 offset:6144\n"
    "v_sub_f32 v220, v144, v132\n"
    "v_mfma_f32_16x16x32_bf16 v[44:47], a[170:173], v[92:95], v[44:47]\n"
    "ds_write_b128 v10, v[228:231] offset:19200\n"
    "v_mfma_f32_16x16x32_bf16 v[48:51], a[178:181], v[92:95], v[48:51]\n"
    "s_waitcnt lgkmcnt(7)\n"
    "v_sub_f32 v221, v145, v133\n"
    "v_cvt_pk_bf16_f32 v232, v220, v221\n"
    "v_mfma_f32_16x16x32_bf16 v[52:55], a[186:189], v[92:95], v[52:55]\n"
    "ds_read_b128 v[92:95], v1 offset:6208\n"
    "v_sub_f32 v222, v146, v134\n"
    "v_mfma_f32_16x16x32_bf16 v[44:47], a[194:197], v[96:99], v[44:47]\n"
    "s_waitcnt lgkmcnt(5)\n"
    "buffer_store_dwordx4 v[252:255], v31, s[24:27], s47 offen offset:0 sc1 nt\n"
    "v_mfma_f32_16x16x32_bf16 v[48:51], a[202:205], v[96:99], v[48:51]\n"
    "v_sub_f32 v223, v147, v135\n"
    "v_cvt_pk_bf16_f32 v233, v222, v223\n"
    "v_mfma_f32_16x16x32_bf16 v[52:55], a[210:213], v[96:99], v[52:55]\n"
    "ds_read_b128 v[96:99], v1 offset:6272\n"
    "s_nop 0\n"
    "v_fma_f32 v56, v44, s44, v56\n"
    "v_mfma_f32_16x16x32_bf16 v[32:35], a[6:9], v[76:79], 0\n"
    "ds_write_b64 v11, v[232:233] offset:19200\n"
    "v_mfma_f32_16x16x32_bf16 v[36:39], a[14:17], v[76:79], 0\n"
    "s_waitcnt lgkmcnt(5)\n"
    "v_fma_f32 v57, v45, s44, v57\n"
    "v_cvt_pk_bf16_f32 v68, v56, v57\n"
    "v_mfma_f32_16x16x32_bf16 v[40:43], a[22:25], v[76:79], 0\n"
    "ds_read_b128 v[76:79], v2 offset:6144\n"
    "v_fma_f32 v58, v46, s44, v58\n"
    "v_mfma_f32_16x16x32_bf16 v[32:35], a[30:33], v[80:83], v[32:35]\n"
    "s_mov_b32 exec_hi, 0\n"
    "buffer_store_dwordx4 v[240:243], v31, s[24:27], s47 offen offset:1024 sc1 nt\n"
    "s_mov_b32 exec_hi, -1\n"
    "v_mfma_f32_16x16x32_bf16 v[36:39], a[38:41], v[80:83], v[36:39]\n"
    "s_waitcnt lgkmcnt(5)\n"
    "v_fma_f32 v59, v47, s44, v59\n"
    "v_cvt_pk_bf16_f32 v69, v58, v59\n"
    "v_mfma_f32_16x16x32_bf16 v[40:43], a[46:49], v[80:83], v[40:43]\n"
    "ds_read_b128 v[80:83], v2 offset:6208\n"
    "v_fma_f32 v60, v48, s44, v60\n"
    "v_mfma_f32_16x16x32_bf16 v[32:35], a[54:57], v[84:87], v[32:35]\n"
    "s_waitcnt lgkmcnt(4)\n"
    "v_fma_f32 v61, v49, s44, v61\n"
    "v_cvt_pk_bf16_f32 v70, v60, v61\n"
    "v_mfma_f32_16x16x32_bf16 v[36:39], a[62:65], v[84:87], v[36:39]\n"
    "v_fma_f32 v62, v50, s44, v62\n"
    "v_fma_f32 v63, v51, s44, v63\n"
    "v_mfma_f32_16x16x32_bf16 v[40:43], a[70:73], v[84:87], v[40:43]\n"
    "ds_read_b128 v[84:87], v2 offset:6272\n"
    "v_cvt_pk_bf16_f32 v71, v62, v63\n"
    "v_mfma_f32_16x16x32_bf16 v[32:35], a[78:81], v[88:91], v[32:35]\n"
    "ds_write_b128 v27, v[68:71] offset:6144\n"
    "v_mfma_f32_16x16x32_bf16 v[36:39], a[86:89], v[88:91], v[36:39]\n"
    "s_waitcnt lgkmcnt(5)\n"
    "v_fma_f32 v64, v52, s44, v64\n"
    "v_fma_f32 v65, v53, s44, v65\n"
    "v_mfma_f32_16x16x32_bf16 v[40:43], a[94:97], v[88:91], v[40:43]\n"
    "ds_read_b128 v[88:91], v3 offset:6144\n"
    "v_cvt_pk_bf16_f32 v72, v64, v65\n"
    "v_mfma_f32_16x16x32_bf16 v[32:35], a[102:105], v[92:95], v[32:35]\n"
    "s_waitcnt lgkmcnt(4)\n"
    "v_fma_f32 v66, v54, s44, v66\n"
    "v_fma_f32 v67, v55, s44, v67\n"
    "v_mfma_f32_16x16x32_bf16 v[36:39], a[110:113], v[92:95], v[36:39]\n"
    "v_cvt_pk_bf16_f32 v73, v66, v67\n"
    "v_sub_f32 v224, v124, v148\n"
    "v_mfma_f32_16x16x32_bf16 v[40:43], a[118:121], v[92:95], v[40:43]\n"
    "ds_read_b128 v[92:95], v3 offset:6208\n"
    "v_sub_f32 v225, v125, v149\n"
    "v_mfma_f32_16x16x32_bf16 v[32:35], a[126:129], v[96:99], v[32:35]\n"
    "ds_write_b64 v28, v[72:73] offset:6144\n"
    "v_mfma_f32_16x16x32_bf16 v[36:39], a[134:137], v[96:99], v[36:39]\n"
    "s_waitcnt lgkmcnt(5)\n"
    "v_cvt_pk_bf16_f32 v234, v224, v225\n"
    "v_sub_f32 v226, v126, v150\n"
    "v_mfma_f32_16x16x32_bf16 v[40:43], a[142:145], v[96:99], v[40:43]\n"
    "ds_read_b128 v[96:99], v3 offset:6272\n"
    "v_sub_f32 v227, v127, v151\n"
    "v_mfma_f32_16x16x32_bf16 v[32:35], a[150:153], v[76:79], v[32:35]\n"
    "s_waitcnt lgkmcnt(5)\n"
    "v_cvt_pk_bf16_f32 v235, v226, v227\n"
    "v_sub_f32 v220, v128, v152\n"
    "v_mfma_f32_16x16x32_bf16 v[36:39], a[158:161], v[76:79], v[36:39]\n"
    "v_sub_f32 v221, v129, v153\n"
    "v_cvt_pk_bf16_f32 v236, v220, v221\n"
    "v_mfma_f32_16x16x32_bf16 v[40:43], a[166:169], v[76:79], v[40:43]\n"
    "ds_read_b128 v[76:79], v4 offset:6144\n"
    "v_sub_f32 v222, v130, v154\n"
    "v_mfma_f32_16x16x32_bf16 v[32:35], a[174:177], v[80:83], v[32:35]\n"
    "v_sub_f32 v223, v131, v155\n"
    "v_cvt_pk_bf16_f32 v237, v222, v223\n"
    "v_mfma_f32_16x16x32_bf16 v[36:39], a[182:185], v[80:83], v[36:39]\n"
    "ds_write_b128 v10, v[234:237] offset:28800\n"
    "v_mfma_f32_16x16x32_bf16 v[40:43], a[190:193], v[80:83], v[40:43]\n"
    "s_waitcnt lgkmcnt(5)\n"
    "ds_read_b128 v[80:83], v4 offset:6208\n"
    "v_mfma_f32_16x16x32_bf16 v[32:35], a[198:201], v[84:87], v[32:35]\n"
    "s_waitcnt lgkmcnt(5)\n"
    "v_sub_f32 v224, v132, v156\n"
    "v_sub_f32 v225, v133, v157\n"
    "v_mfma_f32_16x16x32_bf16 v[36:39], a[206:209], v[84:87], v[36:39]\n"
    "v_cvt_pk_bf16_f32 v238, v224, v225\n"
    "v_sub_f32 v226, v134, v158\n"
    "v_mfma_f32_16x16x32_bf16 v[40:43], a[214:217], v[84:87], v[40:43]\n"
    "ds_read_b128 v[84:87], v4 offset:6272\n"
    "v_sub_f32 v227, v135, v159\n"
    "v_cvt_pk_bf16_f32 v239, v226, v227\n"
    "ds_write_b64 v11, v[238:239] offset:28800\n"
    "s_waitcnt vmcnt(12) lgkmcnt(5)\n"
    "v_lshlrev_b32 v160, 16, v208\n"
    "v_and_b32 v161, 0xffff0000, v208\n"
    "v_lshlrev_b32 v162, 16, v209\n"
    "v_and_b32 v163, 0xffff0000, v209\n"
    "v_lshlrev_b32 v164, 16, v210\n"
    "v_and_b32 v165, 0xffff0000, v210\n"
    "v_mfma_f32_16x16x32_bf16 v[160:163], a[220:223], v[100:103], v[160:163]\n"
    "ds_write_b128 v16, v[32:35] offset:0\n"
    "v_lshlrev_b32 v166, 16, v211\n"
    "v_and_b32 v167, 0xffff0000, v211\n"
    "s_waitcnt vmcnt(11) lgkmcnt(4)\n"
    "v_lshlrev_b32 v168, 16, v212\n"
    "v_mfma_f32_16x16x32_bf16 v[164:167], a[224:227], v[100:103], v[164:167]\n"
    "s_add_u32 s54, s49, 0x1b000\n"
    "buffer_load_dwordx4 v[208:211], v25, s[20:23], s54 offen offset:0 \n"
    "v_and_b32 v169, 0xffff0000, v212\n"
    "v_lshlrev_b32 v170, 16, v213\n"
    "v_and_b32 v171, 0xffff0000, v213\n"
    "ds_write_b128 v16, v[36:39] offset:1024\n"
    "s_nop 0\n"
    "v_mfma_f32_16x16x32_bf16 v[168:171], a[228:231], v[100:103], v[168:171]\n"
    "s_waitcnt lgkmcnt(4)\n"
    "v_mfma_f32_16x16x32_bf16 v[160:163], a[232:235], v[104:107], v[160:163]\n"
    "s_waitcnt lgkmcnt(3)\n"
    "v_mfma_f32_16x16x32_bf16 v[164:167], a[236:239], v[104:107], v[164:167]\n"
    "ds_write_b128 v16, v[40:43] offset:2048\n"
    "v_mfma_f32_16x16x32_bf16 v[168:171], a[240:243], v[104:107], v[168:171]\n"
    "s_add_u32 s55, s49, 0x1b000\n"
    "buffer_load_dwordx2 v[212:213], v26, s[20:23], s55 offen offset:64 \n"
    "v_mfma_f32_16x16x32_bf16 v[160:163], a[244:247], v[108:111], v[160:163]\n"
    "v_mfma_f32_16x16x32_bf16 v[164:167], a[248:251], v[108:111], v[164:167]\n"
    "v_mfma_f32_16x16x32_bf16 v[168:171], a[252:255], v[108:111], v[168:171]\n"
    "v_mfma_f32_16x16x32_bf16 v[44:47], a[2:5], v[88:91], v[32:35]\n"
    "s_nop 4\n"
    "v_sub_f32 v220, v172, v160\n"
    "s_waitcnt vmcnt(8) lgkmcnt(0)\n"
    "s_barrier\n"
    "v_mfma_f32_16x16x32_bf16 v[48:51], a[10:13], v[88:91], v[36:39]\n"
    "ds_read_b128 v[112:115], v6 offset:18432\n"
    "v_sub_f32 v221, v173, v161\n"
    "v_mfma_f32_16x16x32_bf16 v[52:55], a[18:21], v[88:91], v[40:43]\n"
    "ds_read_b128 v[88:91], v5 offset:6144\n"
    "v_cvt_pk_bf16_f32 v228, v220, v221\n"
    "v_mfma_f32_16x16x32_bf16 v[44:47], a[26:29], v[92:95], v[44:47]\n"
    "ds_read_b128 v[116:119], v6 offset:18496\n"
    "v_sub_f32 v222, v174, v162\n"
    "v_mfma_f32_16x16x32_bf16 v[48:51], a[34:37], v[92:95], v[48:51]\n"
    "ds_read_b128 v[120:123], v6 offset:18560\n"
    "v_sub_f32 v223, v175, v163\n"
    "v_mfma_f32_16x16x32_bf16 v[52:55], a[42:45], v[92:95], v[52:55]\n"
    "ds_read_b128 v[92:95], v5 offset:6208\n"
    "v_cvt_pk_bf16_f32 v229, v222, v223\n"
    "v_mfma_f32_16x16x32_bf16 v[44:47], a[50:53], v[96:99], v[44:47]\n"
    "ds_read_b128 v[56:59], v17 offset:0\n"
    "v_sub_f32 v224, v176, v164\n"
    "v_mfma_f32_16x16x32_bf16 v[48:51], a[58:61], v[96:99], v[48:51]\n"
    "ds_read_b128 v[60:63], v17 offset:1024\n"
    "v_sub_f32 v225, v177, v165\n"
    "v_mfma_f32_16x16x32_bf16 v[52:55], a[66:69], v[96:99], v[52:55]\n"
    "ds_read_b128 v[96:99], v5 offset:6272\n"
    "v_cvt_pk_bf16_f32 v230, v224, v225\n"
    "v_mfma_f32_16x16x32_bf16 v[44:47], a[74:77], v[76:79], v[44:47]\n"
    "ds_read_b128 v[64:67], v17 offset:2048\n"
    "v_sub_f32 v226, v178, v166\n"
    "v_mfma_f32_16x16x32_bf16 v[48:51], a[82:85], v[76:79], v[48:51]\n"
    "ds_read_b128 v[252:255], v248 offset:6144\n"
    "v_sub_f32 v227, v179, v167\n"
    "v_mfma_f32_16x16x32_bf16 v[52:55], a[90:93], v[76:79], v[52:55]\n"
    "ds_read_b128 v[76:79], v0 offset:38400\n"
    "v_cvt_pk_bf16_f32 v231, v226, v227\n"
    "v_mfma_f32_16x16x32_bf16 v[44:47], a[98:101], v[80:83], v[44:47]\n"
    "ds_read_b128 v[240:243], v249 offset:6144\n"
    "v_sub_f32 v220, v180, v168\n"
    "v_mfma_f32_16x16x32_bf16 v[48:51], a[106:109], v[80:83], v[48:51]\n"
    "s_add_u32 m0, s45, 0x1bc00\n"
    "s_add_u32 s55, s48, 0x7e000\n"
    "buffer_load_dwordx4 v18, s[16:19], s55 offen nt lds\n"
    "v_mfma_f32_16x16x32_bf16 v[52:55], a[114:117], v[80:83], v[52:55]\n"
    "ds_read_b128 v[80:83], v0 offset:38464\n"
    "v_sub_f32 v221, v181, v169\n"
    "v_mfma_f32_16x16x32_bf16 v[44:47], a[122:125], v[84:87], v[44:47]\n"
    "ds_read_b128 v[100:103], v6 offset:27648\n"
    "v_cvt_pk_bf16_f32 v232, v220, v221\n"
    "v_mfma_f32_16x16x32_bf16 v[48:51], a[130:133], v[84:87], v[48:51]\n"
    "ds_read_b128 v[104:107], v6 offset:27712\n"
    "v_sub_f32 v222, v182, v170\n"
    "v_mfma_f32_16x16x32_bf16 v[52:55], a[138:141], v[84:87], v[52:55]\n"
    "s_waitcnt lgkmcnt(9)\n"
    "ds_read_b128 v[84:87], v0 offset:38528\n"
    "v_mfma_f32_16x16x32_bf16 v[44:47], a[146:149], v[88:91], v[44:47]\n"
    "s_add_u32 m0, s45, 0x1cc00\n"
    "s_add_u32 s52, s48, 0x7e000\n"
    "buffer_load_dwordx4 v19, s[16:19], s52 offen nt lds\n"
    "v_mfma_f32_16x16x32_bf16 v[48:51], a[154:157], v[88:91], v[48:51]\n"
    "ds_read_b128 v[108:111], v6 offset:27776\n"
    "v_sub_f32 v223, v183, v171\n"
    "v_mfma_f32_16x16x32_bf16 v[52:55], a[162:165], v[88:91], v[52:55]\n"
    "ds_read_b128 v[88:91], v1 offset:38400\n"
    "v_cvt_pk_bf16_f32 v233, v222, v223\n"
    "v_mfma_f32_16x16x32_bf16 v[44:47], a[170:173], v[92:95], v[44:47]\n"
    "ds_write_b128 v12, v[228:231] offset:38400\n"
    "v_mfma_f32_16x16x32_bf16 v[48:51], a[178:181], v[92:95], v[48:51]\n"
    "s_waitcnt lgkmcnt(7)\n"
    "s_add_u32 s53, s47, 0xc00\n"
    "buffer_store_dwordx4 v[252:255], v31, s[24:27], s53 offen offset:0 sc1 nt\n"
    "v_mfma_f32_16x16x32_bf16 v[52:55], a[186:189], v[92:95], v[52:55]\n"
    "ds_read_b128 v[92:95], v1 offset:38464\n"
    "v_add_f32_dpp v224, v172, v160 row_ror:8 row_mask:0xf bank_mask:0x3\n"
    "v_mfma_f32_16x16x32_bf16 v[44:47], a[194:197], v[96:99], v[44:47]\n"
    "ds_write_b64 v13, v[232:233] offset:38400\n"
    "v_mfma_f32_16x16x32_bf16 v[48:51], a[202:205], v[96:99], v[48:51]\n"
    "s_waitcnt lgkmcnt(6)\n"
    "v_sub_f32_dpp v224, v160, v172 row_ror:8 row_mask:0xf bank_mask:0xc\n"
    "v_add_f32_dpp v225, v173, v161 row_ror:8 row_mask:0xf bank_mask:0x3\n"
    "v_mfma_f32_16x16x32_bf16 v[52:55], a[210:213], v[96:99], v[52:55]\n"
    "ds_read_b128 v[96:99], v1 offset:38528\n"
    "s_nop 0\n"
    "v_fma_f32 v56, v44, s44, v56\n"
    "v_mfma_f32_16x16x32_bf16 v[32:35], a[6:9], v[76:79], 0\n"
    "s_add_u32 s54, s47, 0xc00\n"
    "s_mov_b32 exec_hi, 0\n"
    "buffer_store_dwordx4 v[240:243], v31, s[24:27], s54 offen offset:1024 sc1 nt\n"
    "s_mov_b32 exec_hi, -1\n"
    "v_mfma_f32_16x16x32_bf16 v[36:39], a[14:17], v[76:79], 0\n"
    "s_waitcnt lgkmcnt(4)\n"
    "v_fma_f32 v57, v45, s44, v57\n"
    "v_cvt_pk_bf16_f32 v68, v56, v57\n"
    "v_mfma_f32_16x16x32_bf16 v[40:43], a[22:25], v[76:79], 0\n"
    "ds_read_b128 v[76:79], v2 offset:38400\n"
    "v_fma_f32 v58, v46, s44, v58\n"
    "v_mfma_f32_16x16x32_bf16 v[32:35], a[30:33], v[80:83], v[32:35]\n"
    "v_fma_f32 v59, v47, s44, v59\n"
    "v_cvt_pk_bf16_f32 v69, v58, v59\n"
    "v_mfma_f32_16x16x32_bf16 v[36:39], a[38:41], v[80:83], v[36:39]\n"
    "s_add_u32 m0, s45, 0x1dc00\n"
    "s_add_u32 s53, s48, 0x7e000\n"
    "buffer_load_dwordx4 v20, s[16:19], s53 offen nt lds\n"
    "v_mfma_f32_16x16x32_bf16 v[40:43], a[46:49], v[80:83], v[40:43]\n"
    "ds_read_b128 v[80:83], v2 offset:38464\n"
    "v_fma_f32 v60, v48, s44, v60\n"
    "v_mfma_f32_16x16x32_bf16 v[32:35], a[54:57], v[84:87], v[32:35]\n"
    "s_waitcnt lgkmcnt(4)\n"
    "v_fma_f32 v61, v49, s44, v61\n"
    "v_cvt_pk_bf16_f32 v70, v60, v61\n"
    "v_mfma_f32_16x16x32_bf16 v[36:39], a[62:65], v[84:87], v[36:39]\n"
    "v_fma_f32 v62, v50, s44, v62\n"
    "v_fma_f32 v63, v51, s44, v63\n"
    "v_mfma_f32_16x16x32_bf16 v[40:43], a[70:73], v[84:87], v[40:43]\n"
    "ds_read_b128 v[84:87], v2 offset:38528\n"
    "v_cvt_pk_bf16_f32 v71, v62, v63\n"
    "v_mfma_f32_16x16x32_bf16 v[32:35], a[78:81], v[88:91], v[32:35]\n"
    "ds_write_b128 v27, v[68:71] offset:0\n"
    "v_mfma_f32_16x16x32_bf16 v[36:39], a[86:89], v[88:91], v[36:39]\n"
    "s_add_u32 m0, s45, 0x1ec00\n"
    "s_add_u32 s54, s48, 0x7e000\n"
    "buffer_load_dwordx4 v21, s[16:19], s54 offen nt lds\n"
    "v_mfma_f32_16x16x32_bf16 v[40:43], a[94:97], v[88:91], v[40:43]\n"
    "ds_read_b128 v[88:91], v3 offset:38400\n"
    "v_fma_f32 v64, v52, s44, v64\n"
    "v_mfma_f32_16x16x32_bf16 v[32:35], a[102:105], v[92:95], v[32:35]\n"
    "s_waitcnt lgkmcnt(4)\n"
    "v_fma_f32 v65, v53, s44, v65\n"
    "v_cvt_pk_bf16_f32 v72, v64, v65\n"
    "v_mfma_f32_16x16x32_bf16 v[36:39], a[110:113], v[92:95], v[36:39]\n"
    "v_fma_f32 v66, v54, s44, v66\n"
    "v_fma_f32 v67, v55, s44, v67\n"
    "v_mfma_f32_16x16x32_bf16 v[40:43], a[118:121], v[92:95], v[40:43]\n"
    "ds_read_b128 v[92:95], v3 offset:38464\n"
    "v_cvt_pk_bf16_f32 v73, v66, v67\n"
    "v_mfma_f32_16x16x32_bf16 v[32:35], a[126:129], v[96:99], v[32:35]\n"
    "ds_write_b64 v28, v[72:73] offset:0\n"
    "v_mfma_f32_16x16x32_bf16 v[36:39], a[134:137], v[96:99], v[36:39]\n"
    "s_add_u32 m0, s46, 0x1fc00\n"
    "s_add_u32 s55, s48, 0x7e000\n"
    "s_mov_b32 exec_hi, 0\n"
    "buffer_load_dwordx4 v22, s[16:19], s55 offen nt lds\n"
    "s_mov_b32 exec_hi, -1\n"
    "v_mfma_f32_16x16x32_bf16 v[40:43], a[142:145], v[96:99], v[40:43]\n"
    "ds_read_b128 v[96:99], v3 offset:38528\n"
    "v_sub_f32_dpp v225, v161, v173 row_ror:8 row_mask:0xf bank_mask:0xc\n"
    "v_mfma_f32_16x16x32_bf16 v[32:35], a[150:153], v[76:79], v[32:35]\n"
    "s_waitcnt lgkmcnt(5)\n"
    "v_cvt_pk_bf16_f32 v234, v224, v225\n"
    "v_add_f32_dpp v226, v174, v162 row_ror:8 row_mask:0xf bank_mask:0x3\n"
    "v_mfma_f32_16x16x32_bf16 v[36:39], a[158:161], v[76:79], v[36:39]\n"
    "v_sub_f32_dpp v226, v162, v174 row_ror:8 row_mask:0xf bank_mask:0xc\n"
    "v_add_f32_dpp v227, v175, v163 row_ror:8 row_mask:0xf bank_mask:0x3\n"
    "v_mfma_f32_16x16x32_bf16 v[40:43], a[166:169], v[76:79], v[40:43]\n"
    "ds_read_b128 v[76:79], v4 offset:38400\n"
    "v_sub_f32_dpp v227, v163, v175 row_ror:8 row_mask:0xf bank_mask:0xc\n"
    "v_mfma_f32_16x16x32_bf16 v[32:35], a[174:177], v[80:83], v[32:35]\n"
    "s_waitcnt lgkmcnt(4)\n"
    "v_cvt_pk_bf16_f32 v235, v226, v227\n"
    "v_add_f32_dpp v220, v176, v164 row_ror:8 row_mask:0xf bank_mask:0x3\n"
    "v_mfma_f32_16x16x32_bf16 v[36:39], a[182:185], v[80:83], v[36:39]\n"
    "v_sub_f32_dpp v220, v164, v176 row_ror:8 row_mask:0xf bank_mask:0xc\n"
    "v_add_f32_dpp v221, v177, v165 row_ror:8 row_mask:0xf bank_mask:0x3\n"
    "v_mfma_f32_16x16x32_bf16 v[40:43], a[190:193], v[80:83], v[40:43]\n"
    "ds_read_b128 v[80:83], v4 offset:38464\n"
    "v_sub_f32_dpp v221, v165, v177 row_ror:8 row_mask:0xf bank_mask:0xc\n"
    "v_mfma_f32_16x16x32_bf16 v[32:35], a[198:201], v[84:87], v[32:35]\n"
    "s_waitcnt lgkmcnt(4)\n"
    "v_cvt_pk_bf16_f32 v236, v220, v221\n"
    "v_add_f32_dpp v222, v178, v166 row_ror:8 row_mask:0xf bank_mask:0x3\n"
    "v_mfma_f32_16x16x32_bf16 v[36:39], a[206:209], v[84:87], v[36:39]\n"
    "v_sub_f32_dpp v222, v166, v178 row_ror:8 row_mask:0xf bank_mask:0xc\n"
    "v_add_f32_dpp v223, v179, v167 row_ror:8 row_mask:0xf bank_mask:0x3\n"
    "v_mfma_f32_16x16x32_bf16 v[40:43], a[214:217], v[84:87], v[40:43]\n"
    "ds_read_b128 v[84:87], v4 offset:38528\n"
    "v_sub_f32_dpp v223, v167, v179 row_ror:8 row_mask:0xf bank_mask:0xc\n"
    "v_mfma_f32_16x16x32_bf16 v[124:127], a[220:223], v[112:115], v[184:187]\n"
    "ds_write_b128 v16, v[32:35] offset:12288\n"
    "v_mfma_f32_16x16x32_bf16 v[128:131], a[224:227], v[112:115], v[188:191]\n"
    "s_waitcnt lgkmcnt(4)\n"
    "v_cvt_pk_bf16_f32 v237, v222, v223\n"
    "v_add_f32_dpp v224, v180, v168 row_ror:8 row_mask:0xf bank_mask:0x3\n"
    "v_mfma_f32_16x16x32_bf16 v[132:135], a[228:231], v[112:115], v[192:195]\n"
    "ds_read_b128 v[112:115], v7 offset:18432\n"
    "v_sub_f32_dpp v224, v168, v180 row_ror:8 row_mask:0xf bank_mask:0xc\n"
    "v_mfma_f32_16x16x32_bf16 v[124:127], a[232:235], v[116:119], v[124:127]\n"
    "ds_write_b128 v14, v[234:237] offset:38400\n"
    "v_mfma_f32_16x16x32_bf16 v[128:131], a[236:239], v[116:119], v[128:131]\n"
    "s_waitcnt lgkmcnt(5)\n"
    "v_add_f32_dpp v225, v181, v169 row_ror:8 row_mask:0xf bank_mask:0x3\n"
    "v_sub_f32_dpp v225, v169, v181 row_ror:8 row_mask:0xf bank_mask:0xc\n"
    "v_mfma_f32_16x16x32_bf16 v[132:135], a[240:243], v[116:119], v[132:135]\n"
    "ds_read_b128 v[116:119], v7 offset:18496\n"
    "v_cvt_pk_bf16_f32 v238, v224, v225\n"
    "v_mfma_f32_16x16x32_bf16 v[124:127], a[244:247], v[120:123], v[124:127]\n"
    "ds_write_b128 v16, v[36:39] offset:13312\n"
    "v_mfma_f32_16x16x32_bf16 v[128:131], a[248:251], v[120:123], v[128:131]\n"
    "s_waitcnt lgkmcnt(6)\n"
    "v_add_f32_dpp v226, v182, v170 row_ror:8 row_mask:0xf bank_mask:0x3\n"
    "v_sub_f32_dpp v226, v170, v182 row_ror:8 row_mask:0xf bank_mask:0xc\n"
    "v_mfma_f32_16x16x32_bf16 v[132:135], a[252:255], v[120:123], v[132:135]\n"
    "ds_read_b128 v[120:123], v7 offset:18560\n"
    "v_add_f32_dpp v227, v183, v171 row_ror:8 row_mask:0xf bank_mask:0x3\n"
    "v_mfma_f32_16x16x32_bf16 v[44:47], a[2:5], v[88:91], v[32:35]\n"
    "ds_write_b128 v16, v[40:43] offset:14336\n"
    "v_mfma_f32_16x16x32_bf16 v[48:51], a[10:13], v[88:91], v[36:39]\n"
    "s_waitcnt lgkmcnt(7)\n"
    "v_sub_f32_dpp v227, v171, v183 row_ror:8 row_mask:0xf bank_mask:0xc\n"
    "v_cvt_pk_bf16_f32 v239, v226, v227\n"
    "v_mfma_f32_16x16x32_bf16 v[52:55], a[18:21], v[88:91], v[40:43]\n"
    "ds_read_b128 v[88:91], v5 offset:38400\n"
    "v_lshlrev_b32 v184, 16, v196\n"
    "v_mfma_f32_16x16x32_bf16 v[44:47], a[26:29], v[92:95], v[44:47]\n"
    "ds_write_b64 v15, v[238:239] offset:38400\n"
    "v_mfma_f32_16x16x32_bf16 v[48:51], a[34:37], v[92:95], v[48:51]\n"
    "s_waitcnt lgkmcnt(7)\n"
    "v_and_b32 v185, 0xffff0000, v196\n"
    "v_lshlrev_b32 v186, 16, v197\n"
    "v_mfma_f32_16x16x32_bf16 v[52:55], a[42:45], v[92:95], v[52:55]\n"
    "ds_read_b128 v[92:95], v5 offset:38464\n"
    "s_waitcnt lgkmcnt(3)\n"
    "s_barrier\n"
    "v_mfma_f32_16x16x32_bf16 v[44:47], a[50:53], v[96:99], v[44:47]\n"
    "ds_read_b128 v[56:59], v17 offset:12288\n"
    "v_and_b32 v187, 0xffff0000, v197\n"
    "v_mfma_f32_16x16x32_bf16 v[48:51], a[58:61], v[96:99], v[48:51]\n"
    "ds_read_b128 v[60:63], v17 offset:13312\n"
    "v_lshlrev_b32 v188, 16, v198\n"
    "v_mfma_f32_16x16x32_bf16 v[52:55], a[66:69], v[96:99], v[52:55]\n"
    "ds_read_b128 v[96:99], v5 offset:38528\n"
    "v_and_b32 v189, 0xffff0000, v198\n"
    "v_mfma_f32_16x16x32_bf16 v[44:47], a[74:77], v[76:79], v[44:47]\n"
    "ds_read_b128 v[64:67], v17 offset:14336\n"
    "v_lshlrev_b32 v190, 16, v199\n"
    "v_mfma_f32_16x16x32_bf16 v[48:51], a[82:85], v[76:79], v[48:51]\n"
    "ds_read_b128 v[252:255], v248 offset:0\n"
    "v_and_b32 v191, 0xffff0000, v199\n"
    "v_mfma_f32_16x16x32_bf16 v[52:55], a[90:93], v[76:79], v[52:55]\n"
    "ds_read_b128 v[76:79], v0 offset:41472\n"
    "v_lshlrev_b32 v192, 16, v200\n"
    "v_mfma_f32_16x16x32_bf16 v[44:47], a[98:101], v[80:83], v[44:47]\n"
    "ds_read_b128 v[240:243], v249 offset:0\n"
    "v_and_b32 v193, 0xffff0000, v200\n"
    "v_mfma_f32_16x16x32_bf16 v[48:51], a[106:109], v[80:83], v[48:51]\n"
    "s_add_u32 s53, s49, 0x2d000\n"
    "buffer_load_dwordx4 v[196:199], v23, s[20:23], s53 offen offset:0 \n"
    "v_mfma_f32_16x16x32_bf16 v[52:55], a[114:117], v[80:83], v[52:55]\n"
    "ds_read_b128 v[80:83], v0 offset:41536\n"
    "v_lshlrev_b32 v194, 16, v201\n"
    "v_mfma_f32_16x16x32_bf16 v[44:47], a[122:125], v[84:87], v[44:47]\n"
    "s_waitcnt lgkmcnt(10)\n"
    "v_and_b32 v195, 0xffff0000, v201\n"
    "v_sub_f32 v220, v136, v124\n"
    "v_mfma_f32_16x16x32_bf16 v[48:51], a[130:133], v[84:87], v[48:51]\n"
    "s_waitcnt lgkmcnt(8)\n"
    "v_sub_f32 v221, v137, v125\n"
    "v_cvt_pk_bf16_f32 v228, v220, v221\n"
    "v_mfma_f32_16x16x32_bf16 v[52:55], a[138:141], v[84:87], v[52:55]\n"
    "ds_read_b128 v[84:87], v0 offset:41600\n"
    "v_sub_f32 v222, v138, v126\n"
    "v_mfma_f32_16x16x32_bf16 v[44:47], a[146:149], v[88:91], v[44:47]\n"
    "s_add_u32 s54, s49, 0x2d000\n"
    "buffer_load_dwordx2 v[200:201], v24, s[20:23], s54 offen offset:64 \n"
    "v_mfma_f32_16x16x32_bf16 v[48:51], a[154:157], v[88:91], v[48:51]\n"
    "s_waitcnt lgkmcnt(6)\n"
    "v_sub_f32 v223, v139, v127\n"
    "v_cvt_pk_bf16_f32 v229, v222, v223\n"
    "v_mfma_f32_16x16x32_bf16 v[52:55], a[162:165], v[88:91], v[52:55]\n"
    "ds_read_b128 v[88:91], v1 offset:41472\n"
    "v_sub_f32 v224, v140, v128\n"
    "v_mfma_f32_16x16x32_bf16 v[44:47], a[170:173], v[92:95], v[44:47]\n"
    "s_waitcnt lgkmcnt(5)\n"
    "v_sub_f32 v225, v141, v129\n"
    "v_cvt_pk_bf16_f32 v230, v224, v225\n"
    "v_mfma_f32_16x16x32_bf16 v[48:51], a[178:181], v[92:95], v[48:51]\n"
    "s_waitcnt lgkmcnt(4)\n"
    "v_sub_f32 v226, v142, v130\n"
    "v_sub_f32 v227, v143, v131\n"
    "v_mfma_f32_16x16x32_bf16 v[52:55], a[186:189], v[92:95], v[52:55]\n"
    "ds_read_b128 v[92:95], v1 offset:41536\n"
    "v_cvt_pk_bf16_f32 v231, v226, v227\n"
    "v_mfma_f32_16x16x32_bf16 v[44:47], a[194:197], v[96:99], v[44:47]\n"
    "s_add_u32 s52, s47, 0x1800\n"
    "buffer_store_dwordx4 v[252:255], v31, s[24:27], s52 offen offset:0 sc1 nt\n"
    "v_mfma_f32_16x16x32_bf16 v[48:51], a[202:205], v[96:99], v[48:51]\n"
    "ds_write_b128 v8, v[228:231] offset:0\n"
    "v_mfma_f32_16x16x32_bf16 v[52:55], a[210:213], v[96:99], v[52:55]\n"
    "s_waitcnt lgkmcnt(4)\n"
    "ds_read_b128 v[96:99], v1 offset:41600\n"
    "v_mfma_f32_16x16x32_bf16 v[32:35], a[6:9], v[76:79], 0\n"
    "s_waitcnt lgkmcnt(4)\n"
    "v_fma_f32 v56, v44, s44, v56\n"
    "v_fma_f32 v57, v45, s44, v57\n"
    "v_mfma_f32_16x16x32_bf16 v[36:39], a[14:17], v[76:79], 0\n"
    "s_add_u32 s53, s47, 0x1800\n"
    "s_mov_b32 exec_hi, 0\n"
    "buffer_store_dwordx4 v[240:243], v31, s[24:27], s53 offen offset:1024 sc1 nt\n"
    "s_mov_b32 exec_hi, -1\n"
    "v_mfma_f32_16x16x32_bf16 v[40:43], a[22:25], v[76:79], 0\n"
    "ds_read_b128 v[76:79], v2 offset:41472\n"
    "v_cvt_pk_bf16_f32 v68, v56, v57\n"
    "v_mfma_f32_16x16x32_bf16 v[32:35], a[30:33], v[80:83], v[32:35]\n"
    "ds_read_b128 v[240:243], v29 offset:1536\n"
    "v_fma_f32 v58, v46, s44, v58\n"
    "v_mfma_f32_16x16x32_bf16 v[36:39], a[38:41], v[80:83], v[36:39]\n"
    "s_waitcnt lgkmcnt(5)\n"
    "v_fma_f32 v59, v47, s44, v59\n"
    "v_cvt_pk_bf16_f32 v69, v58, v59\n"
    "v_mfma_f32_16x16x32_bf16 v[40:43], a[46:49], v[80:83], v[40:43]\n"
    "ds_read_b128 v[80:83], v2 offset:41536\n"
    "v_fma_f32 v60, v48, s44, v60\n"
    "v_mfma_f32_16x16x32_bf16 v[32:35], a[54:57], v[84:87], v[32:35]\n"
    "s_waitcnt lgkmcnt(5)\n"
    "v_fma_f32 v61, v49, s44, v61\n"
    "v_cvt_pk_bf16_f32 v70, v60, v61\n"
    "v_mfma_f32_16x16x32_bf16 v[36:39], a[62:65], v[84:87], v[36:39]\n"
    "v_fma_f32 v62, v50, s44, v62\n"
    "v_fma_f32 v63, v51, s44, v63\n"
    "v_mfma_f32_16x16x32_bf16 v[40:43], a[70:73], v[84:87], v[40:43]\n"
    "ds_read_b128 v[84:87], v2 offset:41600\n"
    "v_cvt_pk_bf16_f32 v71, v62, v63\n"
    "v_mfma_f32_16x16x32_bf16 v[32:35], a[78:81], v[88:91], v[32:35]\n"
    "ds_write_b128 v27, v[68:71] offset:6144\n"
    "v_mfma_f32_16x16x32_bf16 v[36:39], a[86:89], v[88:91], v[36:39]\n"
    "s_waitcnt lgkmcnt(5)\n"
    "v_fma_f32 v64, v52, s44, v64\n"
    "v_fma_f32 v65, v53, s44, v65\n"
    "v_mfma_f32_16x16x32_bf16 v[40:43], a[94:97], v[88:91], v[40:43]\n"
    "ds_read_b128 v[88:91], v3 offset:41472\n"
    "v_cvt_pk_bf16_f32 v72, v64, v65\n"
    "v_mfma_f32_16x16x32_bf16 v[32:35], a[102:105], v[92:95], v[32:35]\n"
    "s_waitcnt lgkmcnt(4)\n"
    "ds_write_b128 v30, v[240:243] offset:0\n"
    "v_mfma_f32_16x16x32_bf16 v[36:39], a[110:113], v[92:95], v[36:39]\n"
    "v_fma_f32 v66, v54, s44, v66\n"
    "v_fma_f32 v67, v55, s44, v67\n"
    "v_mfma_f32_16x16x32_bf16 v[40:43], a[118:121], v[92:95], v[40:43]\n"
    "ds_read_b128 v[92:95], v3 offset:41536\n"
    "v_cvt_pk_bf16_f32 v73, v66, v67\n"
    "v_mfma_f32_16x16x32_bf16 v[32:35], a[126:129], v[96:99], v[32:35]\n"
    "ds_write_b64 v28, v[72:73] offset:6144\n"
    "v_mfma_f32_16x16x32_bf16 v[36:39], a[134:137], v[96:99], v[36:39]\n"
    "s_waitcnt lgkmcnt(6)\n"
    "v_sub_f32 v220, v144, v132\n"
    "v_sub_f32 v221, v145, v133\n"
    "v_mfma_f32_16x16x32_bf16 v[40:43], a[142:145], v[96:99], v[40:43]\n"
    "ds_read_b128 v[96:99], v3 offset:41600\n"
    "v_cvt_pk_bf16_f32 v232, v220, v221\n"
    "v_mfma_f32_16x16x32_bf16 v[32:35], a[150:153], v[76:79], v[32:35]\n"
    "s_waitcnt lgkmcnt(6)\n"
    "v_sub_f32 v222, v146, v134\n"
    "v_sub_f32 v223, v147, v135\n"
    "v_mfma_f32_16x16x32_bf16 v[36:39], a[158:161], v[76:79], v[36:39]\n"
    "v_cvt_pk_bf16_f32 v233, v222, v223\n"
    "v_add_f32 v224, v148, v124\n"
    "v_mfma_f32_16x16x32_bf16 v[40:43], a[166:169], v[76:79], v[40:43]\n"
    "ds_read_b128 v[76:79], v4 offset:41472\n"
    "v_add_f32 v225, v149, v125\n"
    "v_mfma_f32_16x16x32_bf16 v[32:35], a[174:177], v[80:83], v[32:35]\n"
    "ds_write_b64 v9, v[232:233] offset:0\n"
    "v_mfma_f32_16x16x32_bf16 v[36:39], a[182:185], v[80:83], v[36:39]\n"
    "s_waitcnt lgkmcnt(6)\n"
    "v_cvt_pk_bf16_f32 v234, v224, v225\n"
    "v_add_f32 v226, v150, v126\n"
    "v_mfma_f32_16x16x32_bf16 v[40:43], a[190:193], v[80:83], v[40:43]\n"
    "ds_read_b128 v[80:83], v4 offset:41536\n"
    "v_add_f32 v227, v151, v127\n"
    "v_mfma_f32_16x16x32_bf16 v[32:35], a[198:201], v[84:87], v[32:35]\n"
    "v_cvt_pk_bf16_f32 v235, v226, v227\n"
    "v_add_f32 v220, v152, v128\n"
    "v_mfma_f32_16x16x32_bf16 v[36:39], a[206:209], v[84:87], v[36:39]\n"
    "s_waitcnt lgkmcnt(5)\n"
    "v_add_f32 v221, v153, v129\n"
    "v_cvt_pk_bf16_f32 v236, v220, v221\n"
    "v_mfma_f32_16x16x32_bf16 v[40:43], a[214:217], v[84:87], v[40:43]\n"
    "ds_read_b128 v[84:87], v4 offset:41600\n"
    "v_add_f32 v222, v154, v130\n"
    "v_mfma_f32_16x16x32_bf16 v[136:139], a[220:223], v[100:103], v[184:187]\n"
    "ds_write_b128 v16, v[32:35] offset:0\n"
    "v_mfma_f32_16x16x32_bf16 v[140:143], a[224:227], v[100:103], v[188:191]\n"
    "s_waitcnt lgkmcnt(5)\n"
    "v_add_f32 v223, v155, v131\n"
    "v_cvt_pk_bf16_f32 v237, v222, v223\n"
    "v_mfma_f32_16x16x32_bf16 v[144:147], a[228:231], v[100:103], v[192:195]\n"
    "v_add_f32 v224, v156, v132\n"
    "v_add_f32 v225, v157, v133\n"
    "v_mfma_f32_16x16x32_bf16 v[136:139], a[232:235], v[104:107], v[136:139]\n"
    "ds_write_b128 v8, v[234:237] offset:9600\n"
    "v_mfma_f32_16x16x32_bf16 v[140:143], a[236:239], v[104:107], v[140:143]\n"
    "s_waitcnt lgkmcnt(5)\n"
    "v_cvt_pk_bf16_f32 v238, v224, v225\n"
    "v_add_f32 v226, v158, v134\n"
    "v_mfma_f32_16x16x32_bf16 v[144:147], a[240:243], v[104:107], v[144:147]\n"
    "v_add_f32 v227, v159, v135\n"
    "v_cvt_pk_bf16_f32 v239, v226, v227\n"
    "v_mfma_f32_16x16x32_bf16 v[136:139], a[244:247], v[108:111], v[136:139]\n"
    "ds_write_b64 v9, v[238:239] offset:9600\n"
    "v_mfma_f32_16x16x32_bf16 v[140:143], a[248:251], v[108:111], v[140:143]\n"
    "s_waitcnt lgkmcnt(4)\n"
    "v_sub_f32 v220, v124, v148\n"
    "v_sub_f32 v221, v125, v149\n"
    "v_mfma_f32_16x16x32_bf16 v[144:147], a[252:255], v[108:111], v[144:147]\n"
    "s_waitcnt lgkmcnt(3)\n"
    "v_cvt_pk_bf16_f32 v228, v220, v221\n"
    "v_sub_f32 v222, v126, v150\n"
    "v_mfma_f32_16x16x32_bf16 v[44:47], a[2:5], v[88:91], v[32:35]\n"
    "ds_write_b128 v16, v[36:39] offset:1024\n"
    "v_mfma_f32_16x16x32_bf16 v[48:51], a[10:13], v[88:91], v[36:39]\n"
    "v_sub_f32 v223, v127, v151\n"
    "v_cvt_pk_bf16_f32 v229, v222, v223\n"
    "v_mfma_f32_16x16x32_bf16 v[52:55], a[18:21], v[88:91], v[40:43]\n"
    "ds_read_b128 v[88:91], v5 offset:41472\n"
    "v_sub_f32 v224, v128, v152\n"
    "v_mfma_f32_16x16x32_bf16 v[44:47], a[26:29], v[92:95], v[44:47]\n"
    "ds_write_b128 v16, v[40:43] offset:2048\n"
    "v_mfma_f32_16x16x32_bf16 v[48:51], a[34:37], v[92:95], v[48:51]\n"
    "v_sub_f32 v225, v129, v153\n"
    "v_cvt_pk_bf16_f32 v230, v224, v225\n"
    "v_mfma_f32_16x16x32_bf16 v[52:55], a[42:45], v[92:95], v[52:55]\n"
    "ds_read_b128 v[92:95], v5 offset:41536\n"
    "v_sub_f32 v226, v130, v154\n"
    "v_mfma_f32_16x16x32_bf16 v[44:47], a[50:53], v[96:99], v[44:47]\n"
    "v_sub_f32 v227, v131, v155\n"
    "v_cvt_pk_bf16_f32 v231, v226, v227\n"
    "v_mfma_f32_16x16x32_bf16 v[48:51], a[58:61], v[96:99], v[48:51]\n"
    "ds_write_b128 v8, v[228:231] offset:19200\n"
    "v_mfma_f32_16x16x32_bf16 v[52:55], a[66:69], v[96:99], v[52:55]\n"
    "s_waitcnt lgkmcnt(2)\n"
    "s_barrier\n"
    "v_sub_f32 v220, v132, v156\n"
    "v_mfma_f32_16x16x32_bf16 v[44:47], a[74:77], v[76:79], v[44:47]\n"
    "ds_read_b128 v[96:99], v5 offset:41600\n"
    "v_sub_f32 v221, v133, v157\n"
    "v_mfma_f32_16x16x32_bf16 v[48:51], a[82:85], v[76:79], v[48:51]\n"
    "ds_read_b128 v[56:59], v17 offset:0\n"
    "v_cvt_pk_bf16_f32 v232, v220, v221\n"
    "v_mfma_f32_16x16x32_bf16 v[52:55], a[90:93], v[76:79], v[52:55]\n"
    "ds_read_b128 v[76:79], v0 offset:44544\n"
    "v_sub_f32 v222, v134, v158\n"
    "v_mfma_f32_16x16x32_bf16 v[44:47], a[98:101], v[80:83], v[44:47]\n"
    "ds_read_b128 v[60:63], v17 offset:1024\n"
    "v_sub_f32 v223, v135, v159\n"
    "v_mfma_f32_16x16x32_bf16 v[48:51], a[106:109], v[80:83], v[48:51]\n"
    "ds_read_b128 v[64:67], v17 offset:2048\n"
    "v_cvt_pk_bf16_f32 v233, v222, v223\n"
    "v_mfma_f32_16x16x32_bf16 v[52:55], a[114:117], v[80:83], v[52:55]\n"
    "ds_read_b128 v[80:83], v0 offset:44608\n"
    "v_sub_f32 v224, v148, v136\n"
    "v_mfma_f32_16x16x32_bf16 v[44:47], a[122:125], v[84:87], v[44:47]\n"
    "ds_read_b128 v[252:255], v248 offset:6144\n"
    "v_sub_f32 v225, v149, v137\n"
    "v_mfma_f32_16x16x32_bf16 v[48:51], a[130:133], v[84:87], v[48:51]\n"
    "ds_read_b128 v[240:243], v249 offset:6144\n"
    "v_cvt_pk_bf16_f32 v234, v224, v225\n"
    "v_mfma_f32_16x16x32_bf16 v[52:55], a[138:141], v[84:87], v[52:55]\n"
    "ds_read_b128 v[84:87], v0 offset:44672\n"
    "v_sub_f32 v226, v150, v138\n"
    "v_mfma_f32_16x16x32_bf16 v[44:47], a[146:149], v[88:91], v[44:47]\n"
    "ds_write_b64 v9, v[232:233] offset:19200\n"
    "v_mfma_f32_16x16x32_bf16 v[48:51], a[154:157], v[88:91], v[48:51]\n"
    "s_waitcnt lgkmcnt(11)\n"
    "v_sub_f32 v227, v151, v139\n"
    "v_cvt_pk_bf16_f32 v235, v226, v227\n"
    "v_mfma_f32_16x16x32_bf16 v[52:55], a[162:165], v[88:91], v[52:55]\n"
    "ds_read_b128 v[88:91], v1 offset:44544\n"
    "v_sub_f32 v220, v152, v140\n"
    "v_mfma_f32_16x16x32_bf16 v[44:47], a[170:173], v[92:95], v[44:47]\n"
    "s_waitcnt lgkmcnt(9)\n"
    "v_sub_f32 v221, v153, v141\n"
    "v_cvt_pk_bf16_f32 v236, v220, v221\n"
    "v_mfma_f32_16x16x32_bf16 v[48:51], a[178:181], v[92:95], v[48:51]\n"
    "s_waitcnt lgkmcnt(8)\n"
    "v_sub_f32 v222, v154, v142\n"
    "v_sub_f32 v223, v155, v143\n"
    "v_mfma_f32_16x16x32_bf16 v[52:55], a[186:189], v[92:95], v[52:55]\n"
    "ds_read_b128 v[92:95], v1 offset:44608\n"
    "v_cvt_pk_bf16_f32 v237, v222, v223\n"
    "v_mfma_f32_16x16x32_bf16 v[44:47], a[194:197], v[96:99], v[44:47]\n"
    "s_waitcnt lgkmcnt(5)\n"
    "s_add_u32 s52, s47, 0x24000\n"
    "buffer_store_dwordx4 v[252:255], v31, s[24:27], s52 offen offset:0 sc1 nt\n"
    "v_mfma_f32_16x16x32_bf16 v[48:51], a[202:205], v[96:99], v[48:51]\n"
    "ds_write_b128 v8, v[234:237] offset:28800\n"
    "v_mfma_f32_16x16x32_bf16 v[52:55], a[210:213], v[96:99], v[52:55]\n"
    "s_waitcnt lgkmcnt(4)\n"
    "ds_read_b128 v[96:99], v1 offset:44672\n"
    "v_mfma_f32_16x16x32_bf16 v[32:35], a[6:9], v[76:79], 0\n"
    "v_fma_f32 v56, v44, s44, v56\n"
    "v_fma_f32 v57, v45, s44, v57\n"
    "v_mfma_f32_16x16x32_bf16 v[36:39], a[14:17], v[76:79], 0\n"
    "s_add_u32 s53, s47, 0x24000\n"
    "s_mov_b32 exec_hi, 0\n"
    "buffer_store_dwordx4 v[240:243], v31, s[24:27], s53 offen offset:1024 sc1 nt\n"
    "s_mov_b32 exec_hi, -1\n"
    "v_mfma_f32_16x16x32_bf16 v[40:43], a[22:25], v[76:79], 0\n"
    "ds_read_b128 v[76:79], v2 offset:44544\n"
    "v_cvt_pk_bf16_f32 v68, v56, v57\n"
    "v_mfma_f32_16x16x32_bf16 v[32:35], a[30:33], v[80:83], v[32:35]\n"
    "s_waitcnt lgkmcnt(4)\n"
    "v_fma_f32 v58, v46, s44, v58\n"
    "v_fma_f32 v59, v47, s44, v59\n"
    "v_mfma_f32_16x16x32_bf16 v[36:39], a[38:41], v[80:83], v[36:39]\n"
    "v_cvt_pk_bf16_f32 v69, v58, v59\n"
    "v_fma_f32 v60, v48, s44, v60\n"
    "v_mfma_f32_16x16x32_bf16 v[40:43], a[46:49], v[80:83], v[40:43]\n"
    "ds_read_b128 v[80:83], v2 offset:44608\n"
    "v_fma_f32 v61, v49, s44, v61\n"
    "v_mfma_f32_16x16x32_bf16 v[32:35], a[54:57], v[84:87], v[32:35]\n"
    "s_waitcnt lgkmcnt(4)\n"
    "v_cvt_pk_bf16_f32 v70, v60, v61\n"
    "v_fma_f32 v62, v50, s44, v62\n"
    "v_mfma_f32_16x16x32_bf16 v[36:39], a[62:65], v[84:87], v[36:39]\n"
    "v_fma_f32 v63, v51, s44, v63\n"
    "v_cvt_pk_bf16_f32 v71, v62, v63\n"
    "v_mfma_f32_16x16x32_bf16 v[40:43], a[70:73], v[84:87], v[40:43]\n"
    "ds_read_b128 v[84:87], v2 offset:44672\n"
    "v_fma_f32 v64, v52, s44, v64\n"
    "v_mfma_f32_16x16x32_bf16 v[32:35], a[78:81], v[88:91], v[32:35]\n"
    "ds_write_b128 v27, v[68:71] offset:0\n"
    "v_mfma_f32_16x16x32_bf16 v[36:39], a[86:89], v[88:91], v[36:39]\n"
    "s_waitcnt lgkmcnt(4)\n"
    "v_fma_f32 v65, v53, s44, v65\n"
    "v_cvt_pk_bf16_f32 v72, v64, v65\n"
    "v_mfma_f32_16x16x32_bf16 v[40:43], a[94:97], v[88:91], v[40:43]\n"
    "ds_read_b128 v[88:91], v3 offset:44544\n"
    "v_fma_f32 v66, v54, s44, v66\n"
    "v_mfma_f32_16x16x32_bf16 v[32:35], a[102:105], v[92:95], v[32:35]\n"
    "s_waitcnt lgkmcnt(4)\n"
    "v_fma_f32 v67, v55, s44, v67\n"
    "v_cvt_pk_bf16_f32 v73, v66, v67\n"
    "v_mfma_f32_16x16x32_bf16 v[36:39], a[110:113], v[92:95], v[36:39]\n"
    "ds_write_b64 v28, v[72:73] offset:0\n"
    "v_mfma_f32_16x16x32_bf16 v[40:43], a[118:121], v[92:95], v[40:43]\n"
    "v_sub_f32 v224, v156, v144\n"
    "v_sub_f32 v225, v157, v145\n"
    "v_mfma_f32_16x16x32_bf16 v[32:35], a[126:129], v[96:99], v[32:35]\n"
    "ds_read_b128 v[92:95], v3 offset:44608\n"
    "v_cvt_pk_bf16_f32 v238, v224, v225\n"
    "v_mfma_f32_16x16x32_bf16 v[36:39], a[134:137], v[96:99], v[36:39]\n"
    "s_waitcnt lgkmcnt(5)\n"
    "v_sub_f32 v226, v158, v146\n"
    "v_sub_f32 v227, v159, v147\n"
    "v_mfma_f32_16x16x32_bf16 v[40:43], a[142:145], v[96:99], v[40:43]\n"
    "ds_read_b128 v[96:99], v3 offset:44672\n"
    "v_cvt_pk_bf16_f32 v239, v226, v227\n"
    "v_mfma_f32_16x16x32_bf16 v[32:35], a[150:153], v[76:79], v[32:35]\n"
    "ds_write_b64 v9, v[238:239] offset:28800\n"
    "v_mfma_f32_16x16x32_bf16 v[36:39], a[158:161], v[76:79], v[36:39]\n"
    "s_waitcnt lgkmcnt(6)\n"
    "s_waitcnt vmcnt(20) lgkmcnt(5)\n"
    "v_lshlrev_b32 v172, 16, v214\n"
    "v_mfma_f32_16x16x32_bf16 v[40:43], a[166:169], v[76:79], v[40:43]\n"
    "ds_read_b128 v[76:79], v4 offset:44544\n"
    "v_and_b32 v173, 0xffff0000, v214\n"
    "v_mfma_f32_16x16x32_bf16 v[32:35], a[174:177], v[80:83], v[32:35]\n"
    "v_lshlrev_b32 v174, 16, v215\n"
    "v_and_b32 v175, 0xffff0000, v215\n"
    "v_mfma_f32_16x16x32_bf16 v[36:39], a[182:185], v[80:83], v[36:39]\n"
    "s_waitcnt lgkmcnt(5)\n"
    "v_lshlrev_b32 v176, 16, v216\n"
    "v_and_b32 v177, 0xffff0000, v216\n"
    "v_mfma_f32_16x16x32_bf16 v[40:43], a[190:193], v[80:83], v[40:43]\n"
    "ds_read_b128 v[80:83], v4 offset:44608\n"
    "v_lshlrev_b32 v178, 16, v217\n"
    "v_mfma_f32_16x16x32_bf16 v[32:35], a[198:201], v[84:87], v[32:35]\n"
    "v_and_b32 v179, 0xffff0000, v217\n"
    "s_waitcnt vmcnt(19) lgkmcnt(5)\n"
    "v_lshlrev_b32 v180, 16, v218\n"
    "v_mfma_f32_16x16x32_bf16 v[36:39], a[206:209], v[84:87], v[36:39]\n"
    "s_add_u32 s53, s49, 0x24000\n"
    "buffer_load_dwordx4 v[214:217], v25, s[20:23], s53 offen offset:0 \n"
    "v_mfma_f32_16x16x32_bf16 v[40:43], a[214:217], v[84:87], v[40:43]\n"
    "ds_read_b128 v[84:87], v4 offset:44672\n"
    "v_and_b32 v181, 0xffff0000, v218\n"
    "v_mfma_f32_16x16x32_bf16 v[172:175], a[220:223], v[112:115], v[172:175]\n"
    "ds_write_b128 v16, v[32:35] offset:12288\n"
    "v_mfma_f32_16x16x32_bf16 v[176:179], a[224:227], v[112:115], v[176:179]\n"
    "s_waitcnt lgkmcnt(5)\n"
    "v_lshlrev_b32 v182, 16, v219\n"
    "v_and_b32 v183, 0xffff0000, v219\n"
    "s_nop 1\n"
    "v_mfma_f32_16x16x32_bf16 v[180:183], a[228:231], v[112:115], v[180:183]\n"
    "ds_write_b128 v16, v[36:39] offset:13312\n"
    "v_mfma_f32_16x16x32_bf16 v[172:175], a[232:235], v[116:119], v[172:175]\n"
    "s_add_u32 s54, s49, 0x24000\n"
    "buffer_load_dwordx2 v[218:219], v26, s[20:23], s54 offen offset:64 \n"
    "v_mfma_f32_16x16x32_bf16 v[176:179], a[236:239], v[116:119], v[176:179]\n"
    "s_waitcnt lgkmcnt(4)\n"
    "v_mfma_f32_16x16x32_bf16 v[180:183], a[240:243], v[116:119], v[180:183]\n"
    "ds_write_b128 v16, v[40:43] offset:14336\n"
    "v_mfma_f32_16x16x32_bf16 v[172:175], a[244:247], v[120:123], v[172:175]\n"
    "s_waitcnt lgkmcnt(4)\n"
    "v_mfma_f32_16x16x32_bf16 v[176:179], a[248:251], v[120:123], v[176:179]\n"
    "s_waitcnt lgkmcnt(3)\n"
    "v_mfma_f32_16x16x32_bf16 v[180:183], a[252:255], v[120:123], v[180:183]\n"
    "v_mfma_f32_16x16x32_bf16 v[44:47], a[2:5], v[88:91], v[32:35]\n"
    "s_nop 2\n"
    "v_sub_f32 v220, v160, v172\n"
    "v_sub_f32 v221, v161, v173\n"
    "v_mfma_f32_16x16x32_bf16 v[48:51], a[10:13], v[88:91], v[36:39]\n"
    "v_cvt_pk_bf16_f32 v228, v220, v221\n"
    "s_waitcnt vmcnt(8) lgkmcnt(0)\n"
    "s_barrier\n"
    "v_mfma_f32_16x16x32_bf16 v[52:55], a[18:21], v[88:91], v[40:43]\n"
    "ds_read_b128 v[88:91], v5 offset:44544\n"
    "v_sub_f32 v222, v162, v174\n"
    "v_mfma_f32_16x16x32_bf16 v[44:47], a[26:29], v[92:95], v[44:47]\n"
    "ds_read_b128 v[100:103], v6 offset:0\n"
    "v_sub_f32 v223, v163, v175\n"
    "v_mfma_f32_16x16x32_bf16 v[48:51], a[34:37], v[92:95], v[48:51]\n"
    "ds_read_b128 v[104:107], v6 offset:64\n"
    "v_cvt_pk_bf16_f32 v229, v222, v223\n"
    "v_mfma_f32_16x16x32_bf16 v[52:55], a[42:45], v[92:95], v[52:55]\n"
    "ds_read_b128 v[92:95], v5 offset:44608\n"
    "v_sub_f32 v224, v164, v176\n"
    "v_mfma_f32_16x16x32_bf16 v[44:47], a[50:53], v[96:99], v[44:47]\n"
    "ds_read_b128 v[108:111], v6 offset:128\n"
    "v_sub_f32 v225, v165, v177\n"
    "v_mfma_f32_16x16x32_bf16 v[48:51], a[58:61], v[96:99], v[48:51]\n"
    "ds_read_b128 v[56:59], v17 offset:12288\n"
    "v_cvt_pk_bf16_f32 v230, v224, v225\n"
    "v_mfma_f32_16x16x32_bf16 v[52:55], a[66:69], v[96:99], v[52:55]\n"
    "ds_read_b128 v[96:99], v5 offset:44672\n"
    "v_sub_f32 v226, v166, v178\n"
    "v_mfma_f32_16x16x32_bf16 v[44:47], a[74:77], v[76:79], v[44:47]\n"
    "ds_read_b128 v[60:63], v17 offset:13312\n"
    "v_sub_f32 v227, v167, v179\n"
    "v_mfma_f32_16x16x32_bf16 v[48:51], a[82:85], v[76:79], v[48:51]\n"
    "ds_read_b128 v[64:67], v17 offset:14336\n"
    "v_cvt_pk_bf16_f32 v231, v226, v227\n"
    "v_mfma_f32_16x16x32_bf16 v[52:55], a[90:93], v[76:79], v[52:55]\n"
    "ds_read_b128 v[76:79], v0 offset:0\n"
    "v_sub_f32 v220, v168, v180\n"
    "v_mfma_f32_16x16x32_bf16 v[44:47], a[98:101], v[80:83], v[44:47]\n"
    "ds_read_b128 v[252:255], v248 offset:0\n"
    "v_sub_f32 v221, v169, v181\n"
    "v_mfma_f32_16x16x32_bf16 v[48:51], a[106:109], v[80:83], v[48:51]\n"
    "ds_read_b128 v[240:243], v249 offset:0\n"
    "v_cvt_pk_bf16_f32 v232, v220, v221\n"
    "v_mfma_f32_16x16x32_bf16 v[52:55], a[114:117], v[80:83], v[52:55]\n"
    "ds_read_b128 v[80:83], v0 offset:64\n"
    "v_sub_f32 v222, v170, v182\n"
    "v_mfma_f32_16x16x32_bf16 v[44:47], a[122:125], v[84:87], v[44:47]\n"
    "s_add_u32 m0, s45, 0x20400\n"
    "s_add_u32 s54, s48, 0xa2000\n"
    "buffer_load_dwordx4 v18, s[16:19], s54 offen nt lds\n"
    "v_mfma_f32_16x16x32_bf16 v[48:51], a[130:133], v[84:87], v[48:51]\n"
    "ds_read_b128 v[112:115], v6 offset:9216\n"
    "v_sub_f32 v223, v171, v183\n"
    "v_mfma_f32_16x16x32_bf16 v[52:55], a[138:141], v[84:87], v[52:55]\n"
    "s_waitcnt lgkmcnt(9)\n"
    "ds_read_b128 v[84:87], v0 offset:128\n"
    "v_mfma_f32_16x16x32_bf16 v[44:47], a[146:149], v[88:91], v[44:47]\n"
    "ds_read_b128 v[116:119], v6 offset:9280\n"
    "v_cvt_pk_bf16_f32 v233, v222, v223\n"
    "v_mfma_f32_16x16x32_bf16 v[48:51], a[154:157], v[88:91], v[48:51]\n"
    "ds_read_b128 v[120:123], v6 offset:9344\n"
    "v_add_f32_dpp v224, v160, v172 row_ror:8 row_mask:0xf bank_mask:0x3\n"
    "v_mfma_f32_16x16x32_bf16 v[52:55], a[162:165], v[88:91], v[52:55]\n"
    "ds_read_b128 v[88:91], v1 offset:0\n"
    "v_sub_f32_dpp v224, v172, v160 row_ror:8 row_mask:0xf bank_mask:0xc\n"
    "v_mfma_f32_16x16x32_bf16 v[44:47], a[170:173], v[92:95], v[44:47]\n"
    "s_waitcnt lgkmcnt(7)\n"
    "s_add_u32 s52, s47, 0x24c00\n"
    "buffer_store_dwordx4 v[252:255], v31, s[24:27], s52 offen offset:0 sc1 nt\n"
    "v_mfma_f32_16x16x32_bf16 v[48:51], a[178:181], v[92:95], v[48:51]\n"
    "ds_write_b128 v12, v[228:231] offset:0\n"
    "v_mfma_f32_16x16x32_bf16 v[52:55], a[186:189], v[92:95], v[52:55]\n"
    "s_waitcnt lgkmcnt(6)\n"
    "ds_read_b128 v[92:95], v1 offset:64\n"
    "v_mfma_f32_16x16x32_bf16 v[44:47], a[194:197], v[96:99], v[44:47]\n"
    "v_add_f32_dpp v225, v161, v173 row_ror:8 row_mask:0xf bank_mask:0x3\n"
    "v_sub_f32_dpp v225, v173, v161 row_ror:8 row_mask:0xf bank_mask:0xc\n"
    "v_mfma_f32_16x16x32_bf16 v[48:51], a[202:205], v[96:99], v[48:51]\n"
    "s_add_u32 s53, s47, 0x24c00\n"
    "s_mov_b32 exec_hi, 0\n"
    "buffer_store_dwordx4 v[240:243], v31, s[24:27], s53 offen offset:1024 sc1 nt\n"
    "s_mov_b32 exec_hi, -1\n"
    "v_mfma_f32_16x16x32_bf16 v[52:55], a[210:213], v[96:99], v[52:55]\n"
    "ds_read_b128 v[96:99], v1 offset:128\n"
    "v_fma_f32 v56, v44, s44, v56\n"
    "v_mfma_f32_16x16x32_bf16 v[32:35], a[6:9], v[76:79], 0\n"
    "ds_write_b64 v13, v[232:233] offset:0\n"
    "v_mfma_f32_16x16x32_bf16 v[36:39], a[14:17], v[76:79], 0\n"
    "s_waitcnt lgkmcnt(4)\n"
    "v_fma_f32 v57, v45, s44, v57\n"
    "v_cvt_pk_bf16_f32 v68, v56, v57\n"
    "v_mfma_f32_16x16x32_bf16 v[40:43], a[22:25], v[76:79], 0\n"
    "ds_read_b128 v[76:79], v2 offset:0\n"
    "v_fma_f32 v58, v46, s44, v58\n"
    "v_mfma_f32_16x16x32_bf16 v[32:35], a[30:33], v[80:83], v[32:35]\n"
    "s_add_u32 m0, s45, 0x21400\n"
    "s_add_u32 s55, s48, 0xa2000\n"
    "buffer_load_dwordx4 v19, s[16:19], s55 offen nt lds\n"
    "v_mfma_f32_16x16x32_bf16 v[36:39], a[38:41], v[80:83], v[36:39]\n"
    "v_fma_f32 v59, v47, s44, v59\n"
    "v_cvt_pk_bf16_f32 v69, v58, v59\n"
    "v_mfma_f32_16x16x32_bf16 v[40:43], a[46:49], v[80:83], v[40:43]\n"
    "ds_read_b128 v[80:83], v2 offset:64\n"
    "v_fma_f32 v60, v48, s44, v60\n"
    "v_mfma_f32_16x16x32_bf16 v[32:35], a[54:57], v[84:87], v[32:35]\n"
    "s_waitcnt lgkmcnt(4)\n"
    "v_fma_f32 v61, v49, s44, v61\n"
    "v_cvt_pk_bf16_f32 v70, v60, v61\n"
    "v_mfma_f32_16x16x32_bf16 v[36:39], a[62:65], v[84:87], v[36:39]\n"
    "v_fma_f32 v62, v50, s44, v62\n"
    "v_fma_f32 v63, v51, s44, v63\n"
    "v_mfma_f32_16x16x32_bf16 v[40:43], a[70:73], v[84:87], v[40:43]\n"
    "ds_read_b128 v[84:87], v2 offset:128\n"
    "v_cvt_pk_bf16_f32 v71, v62, v63\n"
    "v_mfma_f32_16x16x32_bf16 v[32:35], a[78:81], v[88:91], v[32:35]\n"
    "ds_write_b128 v27, v[68:71] offset:6144\n"
    "v_mfma_f32_16x16x32_bf16 v[36:39], a[86:89], v[88:91], v[36:39]\n"
    "s_add_u32 m0, s45, 0x22400\n"
    "s_add_u32 s52, s48, 0xa2000\n"
    "buffer_load_dwordx4 v20, s[16:19], s52 offen nt lds\n"
    "v_mfma_f32_16x16x32_bf16 v[40:43], a[94:97], v[88:91], v[40:43]\n"
    "ds_read_b128 v[88:91], v3 offset:0\n"
    "v_fma_f32 v64, v52, s44, v64\n"
    "v_mfma_f32_16x16x32_bf16 v[32:35], a[102:105], v[92:95], v[32:35]\n"
    "s_waitcnt lgkmcnt(4)\n"
    "v_fma_f32 v65, v53, s44, v65\n"
    "v_cvt_pk_bf16_f32 v72, v64, v65\n"
    "v_mfma_f32_16x16x32_bf16 v[36:39], a[110:113], v[92:95], v[36:39]\n"
    "v_fma_f32 v66, v54, s44, v66\n"
    "v_fma_f32 v67, v55, s44, v67\n"
    "v_mfma_f32_16x16x32_bf16 v[40:43], a[118:121], v[92:95], v[40:43]\n"
    "ds_read_b128 v[92:95], v3 offset:64\n"
    "v_cvt_pk_bf16_f32 v73, v66, v67\n"
    "v_mfma_f32_16x16x32_bf16 v[32:35], a[126:129], v[96:99], v[32:35]\n"
    "ds_write_b64 v28, v[72:73] offset:6144\n"
    "v_mfma_f32_16x16x32_bf16 v[36:39], a[134:137], v[96:99], v[36:39]\n"
    "s_add_u32 m0, s45, 0x23400\n"
    "s_add_u32 s53, s48, 0xa2000\n"
    "buffer_load_dwordx4 v21, s[16:19], s53 offen nt lds\n"
    "v_mfma_f32_16x16x32_bf16 v[40:43], a[142:145], v[96:99], v[40:43]\n"
    "ds_read_b128 v[96:99], v3 offset:128\n"
    "v_cvt_pk_bf16_f32 v234, v224, v225\n"
    "v_mfma_f32_16x16x32_bf16 v[32:35], a[150:153], v[76:79], v[32:35]\n"
    "s_waitcnt lgkmcnt(5)\n"
    "v_add_f32_dpp v226, v162, v174 row_ror:8 row_mask:0xf bank_mask:0x3\n"
    "v_sub_f32_dpp v226, v174, v162 row_ror:8 row_mask:0xf bank_mask:0xc\n"
    "v_mfma_f32_16x16x32_bf16 v[36:39], a[158:161], v[76:79], v[36:39]\n"
    "v_add_f32_dpp v227, v163, v175 row_ror:8 row_mask:0xf bank_mask:0x3\n"
    "v_sub_f32_dpp v227, v175, v163 row_ror:8 row_mask:0xf bank_mask:0xc\n"
    "v_mfma_f32_16x16x32_bf16 v[40:43], a[166:169], v[76:79], v[40:43]\n"
    "ds_read_b128 v[76:79], v4 offset:0\n"
    "v_cvt_pk_bf16_f32 v235, v226, v227\n"
    "v_mfma_f32_16x16x32_bf16 v[32:35], a[174:177], v[80:83], v[32:35]\n"
    "s_add_u32 m0, s46, 0x24400\n"
    "s_add_u32 s54, s48, 0xa2000\n"
    "s_mov_b32 exec_hi, 0\n"
    "buffer_load_dwordx4 v22, s[16:19], s54 offen nt lds\n"
    "s_mov_b32 exec_hi, -1\n"
    "v_mfma_f32_16x16x32_bf16 v[36:39], a[182:185], v[80:83], v[36:39]\n"
    "s_waitcnt lgkmcnt(4)\n"
    "v_add_f32_dpp v220, v164, v176 row_ror:8 row_mask:0xf bank_mask:0x3\n"
    "v_sub_f32_dpp v220, v176, v164 row_ror:8 row_mask:0xf bank_mask:0xc\n"
    "v_mfma_f32_16x16x32_bf16 v[40:43], a[190:193], v[80:83], v[40:43]\n"
    "ds_read_b128 v[80:83], v4 offset:64\n"
    "v_add_f32_dpp v221, v165, v177 row_ror:8 row_mask:0xf bank_mask:0x3\n"
    "v_mfma_f32_16x16x32_bf16 v[32:35], a[198:201], v[84:87], v[32:35]\n"
    "s_waitcnt lgkmcnt(4)\n"
    "v_sub_f32_dpp v221, v177, v165 row_ror:8 row_mask:0xf bank_mask:0xc\n"
    "v_cvt_pk_bf16_f32 v236, v220, v221\n"
    "v_mfma_f32_16x16x32_bf16 v[36:39], a[206:209], v[84:87], v[36:39]\n"
    "v_add_f32_dpp v222, v166, v178 row_ror:8 row_mask:0xf bank_mask:0x3\n"
    "v_sub_f32_dpp v222, v178, v166 row_ror:8 row_mask:0xf bank_mask:0xc\n"
    "v_mfma_f32_16x16x32_bf16 v[40:43], a[214:217], v[84:87], v[40:43]\n"
    "ds_read_b128 v[84:87], v4 offset:128\n"
    "v_add_f32_dpp v223, v167, v179 row_ror:8 row_mask:0xf bank_mask:0x3\n"
    "v_mfma_f32_16x16x32_bf16 v[148:151], a[220:223], v[100:103], v[184:187]\n"
    "ds_write_b128 v16, v[32:35] offset:0\n"
    "v_mfma_f32_16x16x32_bf16 v[152:155], a[224:227], v[100:103], v[188:191]\n"
    "s_waitcnt lgkmcnt(4)\n"
    "v_sub_f32_dpp v223, v179, v167 row_ror:8 row_mask:0xf bank_mask:0xc\n"
    "v_cvt_pk_bf16_f32 v237, v222, v223\n"
    "v_mfma_f32_16x16x32_bf16 v[156:159], a[228:231], v[100:103], v[192:195]\n"
    "ds_read_b128 v[100:103], v7 offset:0\n"
    "v_add_f32_dpp v224, v168, v180 row_ror:8 row_mask:0xf bank_mask:0x3\n"
    "v_mfma_f32_16x16x32_bf16 v[148:151], a[232:235], v[104:107], v[148:151]\n"
    "ds_write_b128 v14, v[234:237] offset:0\n"
    "v_mfma_f32_16x16x32_bf16 v[152:155], a[236:239], v[104:107], v[152:155]\n"
    "s_waitcnt lgkmcnt(5)\n"
    "v_sub_f32_dpp v224, v180, v168 row_ror:8 row_mask:0xf bank_mask:0xc\n"
    "v_add_f32_dpp v225, v169, v181 row_ror:8 row_mask:0xf bank_mask:0x3\n"
    "v_mfma_f32_16x16x32_bf16 v[156:159], a[240:243], v[104:107], v[156:159]\n"
    "ds_read_b128 v[104:107], v7 offset:64\n"
    "v_sub_f32_dpp v225, v181, v169 row_ror:8 row_mask:0xf bank_mask:0xc\n"
    "v_mfma_f32_16x16x32_bf16 v[148:151], a[244:247], v[108:111], v[148:151]\n"
    "ds_write_b128 v16, v[36:39] offset:1024\n"
    "v_mfma_f32_16x16x32_bf16 v[152:155], a[248:251], v[108:111], v[152:155]\n"
    "s_waitcnt lgkmcnt(6)\n"
    "v_cvt_pk_bf16_f32 v238, v224, v225\n"
    "v_add_f32_dpp v226, v170, v182 row_ror:8 row_mask:0xf bank_mask:0x3\n"
    "v_mfma_f32_16x16x32_bf16 v[156:159], a[252:255], v[108:111], v[156:159]\n"
    "ds_read_b128 v[108:111], v7 offset:128\n"
    "v_sub_f32_dpp v226, v182, v170 row_ror:8 row_mask:0xf bank_mask:0xc\n"
    "v_mfma_f32_16x16x32_bf16 v[44:47], a[2:5], v[88:91], v[32:35]\n"
    "ds_write_b128 v16, v[40:43] offset:2048\n"
    "v_mfma_f32_16x16x32_bf16 v[48:51], a[10:13], v[88:91], v[36:39]\n"
    "s_waitcnt lgkmcnt(7)\n"
    "v_add_f32_dpp v227, v171, v183 row_ror:8 row_mask:0xf bank_mask:0x3\n"
    "v_sub_f32_dpp v227, v183, v171 row_ror:8 row_mask:0xf bank_mask:0xc\n"
    "v_mfma_f32_16x16x32_bf16 v[52:55], a[18:21], v[88:91], v[40:43]\n"
    "ds_read_b128 v[88:91], v5 offset:0\n"
    "v_cvt_pk_bf16_f32 v239, v226, v227\n"
    "v_mfma_f32_16x16x32_bf16 v[44:47], a[26:29], v[92:95], v[44:47]\n"
    "ds_write_b64 v15, v[238:239] offset:0\n"
    "v_mfma_f32_16x16x32_bf16 v[48:51], a[34:37], v[92:95], v[48:51]\n"
    "s_waitcnt lgkmcnt(7)\n"
    "v_lshlrev_b32 v184, 16, v202\n"
    "v_and_b32 v185, 0xffff0000, v202\n"
    "v_mfma_f32_16x16x32_bf16 v[52:55], a[42:45], v[92:95], v[52:55]\n"
    "ds_read_b128 v[92:95], v5 offset:64\n"
    "s_waitcnt lgkmcnt(3)\n"
    "s_barrier\n"
    "v_mfma_f32_16x16x32_bf16 v[44:47], a[50:53], v[96:99], v[44:47]\n"
    "ds_read_b128 v[56:59], v17 offset:0\n"
    "v_lshlrev_b32 v186, 16, v203\n"
    "v_mfma_f32_16x16x32_bf16 v[48:51], a[58:61], v[96:99], v[48:51]\n"
    "ds_read_b128 v[60:63], v17 offset:1024\n"
    "v_and_b32 v187, 0xffff0000, v203\n"
    "v_mfma_f32_16x16x32_bf16 v[52:55], a[66:69], v[96:99], v[52:55]\n"
    "ds_read_b128 v[96:99], v5 offset:128\n"
    "v_lshlrev_b32 v188, 16, v204\n"
    "v_mfma_f32_16x16x32_bf16 v[44:47], a[74:77], v[76:79], v[44:47]\n"
    "ds_read_b128 v[64:67], v17 offset:2048\n"
    "v_and_b32 v189, 0xffff0000, v204\n"
    "v_mfma_f32_16x16x32_bf16 v[48:51], a[82:85], v[76:79], v[48:51]\n"
    "ds_read_b128 v[252:255], v248 offset:6144\n"
    "v_lshlrev_b32 v190, 16, v205\n"
    "v_mfma_f32_16x16x32_bf16 v[52:55], a[90:93], v[76:79], v[52:55]\n"
    "ds_read_b128 v[76:79], v0 offset:3072\n"
    "v_and_b32 v191, 0xffff0000, v205\n"
    "v_mfma_f32_16x16x32_bf16 v[44:47], a[98:101], v[80:83], v[44:47]\n"
    "ds_read_b128 v[240:243], v249 offset:6144\n"
    "v_lshlrev_b32 v192, 16, v206\n"
    "v_mfma_f32_16x16x32_bf16 v[48:51], a[106:109], v[80:83], v[48:51]\n"
    "buffer_load_dwordx4 v[202:205], v23, s[56:59], 0 offen offset:0 \n"
    "v_and_b32 v193, 0xffff0000, v206\n"
    "v_mfma_f32_16x16x32_bf16 v[52:55], a[114:117], v[80:83], v[52:55]\n"
    "ds_read_b128 v[80:83], v0 offset:3136\n"
    "v_lshlrev_b32 v194, 16, v207\n"
    "v_mfma_f32_16x16x32_bf16 v[44:47], a[122:125], v[84:87], v[44:47]\n"
    "s_waitcnt lgkmcnt(10)\n"
    "v_and_b32 v195, 0xffff0000, v207\n"
    "v_sub_f32 v220, v124, v148\n"
    "v_mfma_f32_16x16x32_bf16 v[48:51], a[130:133], v[84:87], v[48:51]\n"
    "s_waitcnt lgkmcnt(8)\n"
    "v_sub_f32 v221, v125, v149\n"
    "v_cvt_pk_bf16_f32 v228, v220, v221\n"
    "v_mfma_f32_16x16x32_bf16 v[52:55], a[138:141], v[84:87], v[52:55]\n"
    "ds_read_b128 v[84:87], v0 offset:3200\n"
    "v_sub_f32 v222, v126, v150\n"
    "v_mfma_f32_16x16x32_bf16 v[44:47], a[146:149], v[88:91], v[44:47]\n"
    "buffer_load_dwordx2 v[206:207], v24, s[56:59], 0 offen offset:64 \n"
    "v_sub_f32 v223, v127, v151\n"
    "v_mfma_f32_16x16x32_bf16 v[48:51], a[154:157], v[88:91], v[48:51]\n"
    "s_waitcnt lgkmcnt(6)\n"
    "v_cvt_pk_bf16_f32 v229, v222, v223\n"
    "v_sub_f32 v224, v128, v152\n"
    "v_mfma_f32_16x16x32_bf16 v[52:55], a[162:165], v[88:91], v[52:55]\n"
    "ds_read_b128 v[88:91], v1 offset:3072\n"
    "v_sub_f32 v225, v129, v153\n"
    "v_mfma_f32_16x16x32_bf16 v[44:47], a[170:173], v[92:95], v[44:47]\n"
    "s_waitcnt lgkmcnt(5)\n"
    "v_cvt_pk_bf16_f32 v230, v224, v225\n"
    "v_sub_f32 v226, v130, v154\n"
    "v_mfma_f32_16x16x32_bf16 v[48:51], a[178:181], v[92:95], v[48:51]\n"
    "s_waitcnt lgkmcnt(4)\n"
    "v_sub_f32 v227, v131, v155\n"
    "v_cvt_pk_bf16_f32 v231, v226, v227\n"
    "v_mfma_f32_16x16x32_bf16 v[52:55], a[186:189], v[92:95], v[52:55]\n"
    "ds_read_b128 v[92:95], v1 offset:3136\n"
    "v_sub_f32 v220, v132, v156\n"
    "v_mfma_f32_16x16x32_bf16 v[44:47], a[194:197], v[96:99], v[44:47]\n"
    "s_add_u32 s55, s47, 0x25800\n"
    "buffer_store_dwordx4 v[252:255], v31, s[24:27], s55 offen offset:0 sc1 nt\n"
    "v_mfma_f32_16x16x32_bf16 v[48:51], a[202:205], v[96:99], v[48:51]\n"
    "ds_write_b128 v10, v[228:231] offset:0\n"
    "v_mfma_f32_16x16x32_bf16 v[52:55], a[210:213], v[96:99], v[52:55]\n"
    "s_waitcnt lgkmcnt(4)\n"
    "ds_read_b128 v[96:99], v1 offset:3200\n"
    "v_mfma_f32_16x16x32_bf16 v[32:35], a[6:9], v[76:79], 0\n"
    "s_waitcnt lgkmcnt(4)\n"
    "v_fma_f32 v56, v44, s44, v56\n"
    "v_fma_f32 v57, v45, s44, v57\n"
    "v_mfma_f32_16x16x32_bf16 v[36:39], a[14:17], v[76:79], 0\n"
    "s_add_u32 s52, s47, 0x25800\n"
    "s_mov_b32 exec_hi, 0\n"
    "buffer_store_dwordx4 v[240:243], v31, s[24:27], s52 offen offset:1024 sc1 nt\n"
    "s_mov_b32 exec_hi, -1\n"
    "v_mfma_f32_16x16x32_bf16 v[40:43], a[22:25], v[76:79], 0\n"
    "ds_read_b128 v[76:79], v2 offset:3072\n"
    "v_cvt_pk_bf16_f32 v68, v56, v57\n"
    "v_mfma_f32_16x16x32_bf16 v[32:35], a[30:33], v[80:83], v[32:35]\n"
    "ds_read_b128 v[240:243], v29 offset:3072\n"
    "v_fma_f32 v58, v46, s44, v58\n"
    "v_mfma_f32_16x16x32_bf16 v[36:39], a[38:41], v[80:83], v[36:39]\n"
    "s_waitcnt lgkmcnt(5)\n"
    "v_fma_f32 v59, v47, s44, v59\n"
    "v_cvt_pk_bf16_f32 v69, v58, v59\n"
    "v_mfma_f32_16x16x32_bf16 v[40:43], a[46:49], v[80:83], v[40:43]\n"
    "ds_read_b128 v[80:83], v2 offset:3136\n"
    "v_fma_f32 v60, v48, s44, v60\n"
    "v_mfma_f32_16x16x32_bf16 v[32:35], a[54:57], v[84:87], v[32:35]\n"
    "s_waitcnt lgkmcnt(5)\n"
    "v_fma_f32 v61, v49, s44, v61\n"
    "v_cvt_pk_bf16_f32 v70, v60, v61\n"
    "v_mfma_f32_16x16x32_bf16 v[36:39], a[62:65], v[84:87], v[36:39]\n"
    "v_fma_f32 v62, v50, s44, v62\n"
    "v_fma_f32 v63, v51, s44, v63\n"
    "v_mfma_f32_16x16x32_bf16 v[40:43], a[70:73], v[84:87], v[40:43]\n"
    "ds_read_b128 v[84:87], v2 offset:3200\n"
    "v_cvt_pk_bf16_f32 v71, v62, v63\n"
    "v_mfma_f32_16x16x32_bf16 v[32:35], a[78:81], v[88:91], v[32:35]\n"
    "ds_write_b128 v27, v[68:71] offset:0\n"
    "v_mfma_f32_16x16x32_bf16 v[36:39], a[86:89], v[88:91], v[36:39]\n"
    "s_waitcnt lgkmcnt(5)\n"
    "v_fma_f32 v64, v52, s44, v64\n"
    "v_fma_f32 v65, v53, s44, v65\n"
    "v_mfma_f32_16x16x32_bf16 v[40:43], a[94:97], v[88:91], v[40:43]\n"
    "ds_read_b128 v[88:91], v3 offset:3072\n"
    "v_cvt_pk_bf16_f32 v72, v64, v65\n"
    "v_mfma_f32_16x16x32_bf16 v[32:35], a[102:105], v[92:95], v[32:35]\n"
    "s_waitcnt lgkmcnt(4)\n"
    "ds_write_b128 v30, v[240:243] offset:38400\n"
    "v_mfma_f32_16x16x32_bf16 v[36:39], a[110:113], v[92:95], v[36:39]\n"
    "v_fma_f32 v66, v54, s44, v66\n"
    "v_fma_f32 v67, v55, s44, v67\n"
    "v_mfma_f32_16x16x32_bf16 v[40:43], a[118:121], v[92:95], v[40:43]\n"
    "ds_read_b128 v[92:95], v3 offset:3136\n"
    "v_cvt_pk_bf16_f32 v73, v66, v67\n"
    "v_mfma_f32_16x16x32_bf16 v[32:35], a[126:129], v[96:99], v[32:35]\n"
    "ds_write_b64 v28, v[72:73] offset:0\n"
    "v_mfma_f32_16x16x32_bf16 v[36:39], a[134:137], v[96:99], v[36:39]\n"
    "s_waitcnt lgkmcnt(6)\n"
    "v_sub_f32 v221, v133, v157\n"
    "v_cvt_pk_bf16_f32 v232, v220, v221\n"
    "v_mfma_f32_16x16x32_bf16 v[40:43], a[142:145], v[96:99], v[40:43]\n"
    "ds_read_b128 v[96:99], v3 offset:3200\n"
    "v_sub_f32 v222, v134, v158\n"
    "v_mfma_f32_16x16x32_bf16 v[32:35], a[150:153], v[76:79], v[32:35]\n"
    "s_waitcnt lgkmcnt(6)\n"
    "v_sub_f32 v223, v135, v159\n"
    "v_cvt_pk_bf16_f32 v233, v222, v223\n"
    "v_mfma_f32_16x16x32_bf16 v[36:39], a[158:161], v[76:79], v[36:39]\n"
    "ds_write_b64 v11, v[232:233] offset:0\n"
    "v_mfma_f32_16x16x32_bf16 v[40:43], a[166:169], v[76:79], v[40:43]\n"
    "v_add_f32 v224, v136, v148\n"
    "v_add_f32 v225, v137, v149\n"
    "v_mfma_f32_16x16x32_bf16 v[32:35], a[174:177], v[80:83], v[32:35]\n"
    "ds_read_b128 v[76:79], v4 offset:3072\n"
    "v_cvt_pk_bf16_f32 v234, v224, v225\n"
    "v_mfma_f32_16x16x32_bf16 v[36:39], a[182:185], v[80:83], v[36:39]\n"
    "s_waitcnt lgkmcnt(6)\n"
    "v_add_f32 v226, v138, v150\n"
    "v_add_f32 v227, v139, v151\n"
    "v_mfma_f32_16x16x32_bf16 v[40:43], a[190:193], v[80:83], v[40:43]\n"
    "ds_read_b128 v[80:83], v4 offset:3136\n"
    "v_cvt_pk_bf16_f32 v235, v226, v227\n"
    "v_mfma_f32_16x16x32_bf16 v[32:35], a[198:201], v[84:87], v[32:35]\n"
    "v_add_f32 v220, v140, v152\n"
    "v_add_f32 v221, v141, v153\n"
    "v_mfma_f32_16x16x32_bf16 v[36:39], a[206:209], v[84:87], v[36:39]\n"
    "s_waitcnt lgkmcnt(5)\n"
    "v_cvt_pk_bf16_f32 v236, v220, v221\n"
    "v_add_f32 v222, v142, v154\n"
    "v_mfma_f32_16x16x32_bf16 v[40:43], a[214:217], v[84:87], v[40:43]\n"
    "ds_read_b128 v[84:87], v4 offset:3200\n"
    "v_add_f32 v223, v143, v155\n"
    "v_mfma_f32_16x16x32_bf16 v[124:127], a[220:223], v[112:115], v[184:187]\n"
    "ds_write_b128 v16, v[32:35] offset:12288\n"
    "v_mfma_f32_16x16x32_bf16 v[128:131], a[224:227], v[112:115], v[188:191]\n"
    "s_waitcnt lgkmcnt(5)\n"
    "v_cvt_pk_bf16_f32 v237, v222, v223\n"
    "v_add_f32 v224, v144, v156\n"
    "v_mfma_f32_16x16x32_bf16 v[132:135], a[228:231], v[112:115], v[192:195]\n"
    "v_add_f32 v225, v145, v157\n"
    "v_cvt_pk_bf16_f32 v238, v224, v225\n"
    "v_mfma_f32_16x16x32_bf16 v[124:127], a[232:235], v[116:119], v[124:127]\n"
    "ds_write_b128 v10, v[234:237] offset:9600\n"
    "v_mfma_f32_16x16x32_bf16 v[128:131], a[236:239], v[116:119], v[128:131]\n"
    "s_waitcnt lgkmcnt(4)\n"
    "v_add_f32 v226, v146, v158\n"
    "v_add_f32 v227, v147, v159\n"
    "v_mfma_f32_16x16x32_bf16 v[132:135], a[240:243], v[116:119], v[132:135]\n"
    "v_cvt_pk_bf16_f32 v239, v226, v227\n"
    "v_sub_f32 v220, v148, v136\n"
    "v_mfma_f32_16x16x32_bf16 v[124:127], a[244:247], v[120:123], v[124:127]\n"
    "ds_write_b64 v11, v[238:239] offset:9600\n"
    "v_mfma_f32_16x16x32_bf16 v[128:131], a[248:251], v[120:123], v[128:131]\n"
    "s_waitcnt lgkmcnt(4)\n"
    "v_sub_f32 v221, v149, v137\n"
    "v_cvt_pk_bf16_f32 v228, v220, v221\n"
    "v_mfma_f32_16x16x32_bf16 v[132:135], a[252:255], v[120:123], v[132:135]\n"
    "s_waitcnt lgkmcnt(3)\n"
    "v_sub_f32 v222, v150, v138\n"
    "v_sub_f32 v223, v151, v139\n"
    "v_mfma_f32_16x16x32_bf16 v[44:47], a[2:5], v[88:91], v[32:35]\n"
    "ds_write_b128 v16, v[36:39] offset:13312\n"
    "v_mfma_f32_16x16x32_bf16 v[48:51], a[10:13], v[88:91], v[36:39]\n"
    "v_cvt_pk_bf16_f32 v229, v222, v223\n"
    "v_sub_f32 v224, v152, v140\n"
    "v_mfma_f32_16x16x32_bf16 v[52:55], a[18:21], v[88:91], v[40:43]\n"
    "ds_read_b128 v[88:91], v5 offset:3072\n"
    "v_sub_f32 v225, v153, v141\n"
    "v_mfma_f32_16x16x32_bf16 v[44:47], a[26:29], v[92:95], v[44:47]\n"
    "ds_write_b128 v16, v[40:43] offset:14336\n"
    "v_mfma_f32_16x16x32_bf16 v[48:51], a[34:37], v[92:95], v[48:51]\n"
    "v_cvt_pk_bf16_f32 v230, v224, v225\n"
    "v_sub_f32 v226, v154, v142\n"
    "v_mfma_f32_16x16x32_bf16 v[52:55], a[42:45], v[92:95], v[52:55]\n"
    "ds_read_b128 v[92:95], v5 offset:3136\n"
    "v_sub_f32 v227, v155, v143\n"
    "v_mfma_f32_16x16x32_bf16 v[44:47], a[50:53], v[96:99], v[44:47]\n"
    "v_cvt_pk_bf16_f32 v231, v226, v227\n"
    "v_sub_f32 v220, v156, v144\n"
    "v_mfma_f32_16x16x32_bf16 v[48:51], a[58:61], v[96:99], v[48:51]\n"
    "ds_write_b128 v10, v[228:231] offset:19200\n"
    "v_mfma_f32_16x16x32_bf16 v[52:55], a[66:69], v[96:99], v[52:55]\n"
    "s_waitcnt lgkmcnt(2)\n"
    "s_barrier\n"
    "v_sub_f32 v221, v157, v145\n"
    "v_mfma_f32_16x16x32_bf16 v[44:47], a[74:77], v[76:79], v[44:47]\n"
    "ds_read_b128 v[96:99], v5 offset:3200\n"
    "v_cvt_pk_bf16_f32 v232, v220, v221\n"
    "v_mfma_f32_16x16x32_bf16 v[48:51], a[82:85], v[76:79], v[48:51]\n"
    "ds_read_b128 v[56:59], v17 offset:12288\n"
    "v_sub_f32 v222, v158, v146\n"
    "v_mfma_f32_16x16x32_bf16 v[52:55], a[90:93], v[76:79], v[52:55]\n"
    "ds_read_b128 v[76:79], v0 offset:6144\n"
    "v_sub_f32 v223, v159, v147\n"
    "v_mfma_f32_16x16x32_bf16 v[44:47], a[98:101], v[80:83], v[44:47]\n"
    "ds_read_b128 v[60:63], v17 offset:13312\n"
    "v_cvt_pk_bf16_f32 v233, v222, v223\n"
    "v_mfma_f32_16x16x32_bf16 v[48:51], a[106:109], v[80:83], v[48:51]\n"
    "ds_read_b128 v[64:67], v17 offset:14336\n"
    "v_sub_f32 v224, v136, v124\n"
    "v_mfma_f32_16x16x32_bf16 v[52:55], a[114:117], v[80:83], v[52:55]\n"
    "ds_read_b128 v[80:83], v0 offset:6208\n"
    "v_sub_f32 v225, v137, v125\n"
    "v_mfma_f32_16x16x32_bf16 v[44:47], a[122:125], v[84:87], v[44:47]\n"
    "ds_read_b128 v[252:255], v248 offset:0\n"
    "v_cvt_pk_bf16_f32 v234, v224, v225\n"
    "v_mfma_f32_16x16x32_bf16 v[48:51], a[130:133], v[84:87], v[48:51]\n"
    "ds_read_b128 v[240:243], v249 offset:0\n"
    "v_sub_f32 v226, v138, v126\n"
    "v_mfma_f32_16x16x32_bf16 v[52:55], a[138:141], v[84:87], v[52:55]\n"
    "ds_read_b128 v[84:87], v0 offset:6272\n"
    "v_sub_f32 v227, v139, v127\n"
    "v_mfma_f32_16x16x32_bf16 v[44:47], a[146:149], v[88:91], v[44:47]\n"
    "ds_write_b64 v11, v[232:233] offset:19200\n"
    "v_mfma_f32_16x16x32_bf16 v[48:51], a[154:157], v[88:91], v[48:51]\n"
    "s_waitcnt lgkmcnt(11)\n"
    "v_cvt_pk_bf16_f32 v235, v226, v227\n"
    "v_sub_f32 v220, v140, v128\n"
    "v_mfma_f32_16x16x32_bf16 v[52:55], a[162:165], v[88:91], v[52:55]\n"
    "ds_read_b128 v[88:91], v1 offset:6144\n"
    "v_sub_f32 v221, v141, v129\n"
    "v_mfma_f32_16x16x32_bf16 v[44:47], a[170:173], v[92:95], v[44:47]\n"
    "s_waitcnt lgkmcnt(9)\n"
    "v_cvt_pk_bf16_f32 v236, v220, v221\n"
    "v_sub_f32 v222, v142, v130\n"
    "v_mfma_f32_16x16x32_bf16 v[48:51], a[178:181], v[92:95], v[48:51]\n"
    "s_waitcnt lgkmcnt(8)\n"
    "v_sub_f32 v223, v143, v131\n"
    "v_cvt_pk_bf16_f32 v237, v222, v223\n"
    "v_mfma_f32_16x16x32_bf16 v[52:55], a[186:189], v[92:95], v[52:55]\n"
    "ds_read_b128 v[92:95], v1 offset:6208\n"
    "v_sub_f32 v224, v144, v132\n"
    "v_mfma_f32_16x16x32_bf16 v[44:47], a[194:197], v[96:99], v[44:47]\n"
    "s_waitcnt lgkmcnt(5)\n"
    "s_add_u32 s55, s47, 0x48000\n"
    "buffer_store_dwordx4 v[252:255], v31, s[24:27], s55 offen offset:0 sc1 nt\n"
    "v_mfma_f32_16x16x32_bf16 v[48:51], a[202:205], v[96:99], v[48:51]\n"
    "ds_write_b128 v10, v[234:237] offset:28800\n"
    "v_mfma_f32_16x16x32_bf16 v[52:55], a[210:213], v[96:99], v[52:55]\n"
    "s_waitcnt lgkmcnt(4)\n"
    "ds_read_b128 v[96:99], v1 offset:6272\n"
    "v_mfma_f32_16x16x32_bf16 v[32:35], a[6:9], v[76:79], 0\n"
    "v_fma_f32 v56, v44, s44, v56\n"
    "v_fma_f32 v57, v45, s44, v57\n"
    "v_mfma_f32_16x16x32_bf16 v[36:39], a[14:17], v[76:79], 0\n"
    "s_add_u32 s52, s47, 0x48000\n"
    "s_mov_b32 exec_hi, 0\n"
    "buffer_store_dwordx4 v[240:243], v31, s[24:27], s52 offen offset:1024 sc1 nt\n"
    "s_mov_b32 exec_hi, -1\n"
    "v_mfma_f32_16x16x32_bf16 v[40:43], a[22:25], v[76:79], 0\n"
    "ds_read_b128 v[76:79], v2 offset:6144\n"
    "v_cvt_pk_bf16_f32 v68, v56, v57\n"
    "v_mfma_f32_16x16x32_bf16 v[32:35], a[30:33], v[80:83], v[32:35]\n"
    "s_waitcnt lgkmcnt(4)\n"
    "v_fma_f32 v58, v46, s44, v58\n"
    "v_fma_f32 v59, v47, s44, v59\n"
    "v_mfma_f32_16x16x32_bf16 v[36:39], a[38:41], v[80:83], v[36:39]\n"
    "v_cvt_pk_bf16_f32 v69, v58, v59\n"
    "v_fma_f32 v60, v48, s44, v60\n"
    "v_mfma_f32_16x16x32_bf16 v[40:43], a[46:49], v[80:83], v[40:43]\n"
    "ds_read_b128 v[80:83], v2 offset:6208\n"
    "v_fma_f32 v61, v49, s44, v61\n"
    "v_mfma_f32_16x16x32_bf16 v[32:35], a[54:57], v[84:87], v[32:35]\n"
    "s_waitcnt lgkmcnt(4)\n"
    "v_cvt_pk_bf16_f32 v70, v60, v61\n"
    "v_fma_f32 v62, v50, s44, v62\n"
    "v_mfma_f32_16x16x32_bf16 v[36:39], a[62:65], v[84:87], v[36:39]\n"
    "v_fma_f32 v63, v51, s44, v63\n"
    "v_cvt_pk_bf16_f32 v71, v62, v63\n"
    "v_mfma_f32_16x16x32_bf16 v[40:43], a[70:73], v[84:87], v[40:43]\n"
    "ds_read_b128 v[84:87], v2 offset:6272\n"
    "v_fma_f32 v64, v52, s44, v64\n"
    "v_mfma_f32_16x16x32_bf16 v[32:35], a[78:81], v[88:91], v[32:35]\n"
    "ds_write_b128 v27, v[68:71] offset:6144\n"
    "v_mfma_f32_16x16x32_bf16 v[36:39], a[86:89], v[88:91], v[36:39]\n"
    "s_waitcnt lgkmcnt(4)\n"
    "v_fma_f32 v65, v53, s44, v65\n"
    "v_cvt_pk_bf16_f32 v72, v64, v65\n"
    "v_mfma_f32_16x16x32_bf16 v[40:43], a[94:97], v[88:91], v[40:43]\n"
    "ds_read_b128 v[88:91], v3 offset:6144\n"
    "v_fma_f32 v66, v54, s44, v66\n"
    "v_mfma_f32_16x16x32_bf16 v[32:35], a[102:105], v[92:95], v[32:35]\n"
    "s_waitcnt lgkmcnt(4)\n"
    "v_fma_f32 v67, v55, s44, v67\n"
    "v_cvt_pk_bf16_f32 v73, v66, v67\n"
    "v_mfma_f32_16x16x32_bf16 v[36:39], a[110:113], v[92:95], v[36:39]\n"
    "ds_write_b64 v28, v[72:73] offset:6144\n"
    "v_mfma_f32_16x16x32_bf16 v[40:43], a[118:121], v[92:95], v[40:43]\n"
    "v_sub_f32 v225, v145, v133\n"
    "v_cvt_pk_bf16_f32 v238, v224, v225\n"
    "v_mfma_f32_16x16x32_bf16 v[32:35], a[126:129], v[96:99], v[32:35]\n"
    "ds_read_b128 v[92:95], v3 offset:6208\n"
    "v_sub_f32 v226, v146, v134\n"
    "v_mfma_f32_16x16x32_bf16 v[36:39], a[134:137], v[96:99], v[36:39]\n"
    "s_waitcnt lgkmcnt(5)\n"
    "v_sub_f32 v227, v147, v135\n"
    "v_cvt_pk_bf16_f32 v239, v226, v227\n"
    "v_mfma_f32_16x16x32_bf16 v[40:43], a[142:145], v[96:99], v[40:43]\n"
    "ds_read_b128 v[96:99], v3 offset:6272\n"
    "v_lshlrev_b32 v160, 16, v208\n"
    "v_mfma_f32_16x16x32_bf16 v[32:35], a[150:153], v[76:79], v[32:35]\n"
    "ds_write_b64 v11, v[238:239] offset:28800\n"
    "v_mfma_f32_16x16x32_bf16 v[36:39], a[158:161], v[76:79], v[36:39]\n"
    "s_waitcnt lgkmcnt(6)\n"
    "v_and_b32 v161, 0xffff0000, v208\n"
    "v_lshlrev_b32 v162, 16, v209\n"
    "v_mfma_f32_16x16x32_bf16 v[40:43], a[166:169], v[76:79], v[40:43]\n"
    "ds_read_b128 v[76:79], v4 offset:6144\n"
    "v_and_b32 v163, 0xffff0000, v209\n"
    "v_mfma_f32_16x16x32_bf16 v[32:35], a[174:177], v[80:83], v[32:35]\n"
    "v_lshlrev_b32 v164, 16, v210\n"
    "v_and_b32 v165, 0xffff0000, v210\n"
    "v_mfma_f32_16x16x32_bf16 v[36:39], a[182:185], v[80:83], v[36:39]\n"
    "s_waitcnt lgkmcnt(5)\n"
    "v_lshlrev_b32 v166, 16, v211\n"
    "v_and_b32 v167, 0xffff0000, v211\n"
    "v_mfma_f32_16x16x32_bf16 v[40:43], a[190:193], v[80:83], v[40:43]\n"
    "ds_read_b128 v[80:83], v4 offset:6208\n"
    "v_lshlrev_b32 v168, 16, v212\n"
    "v_mfma_f32_16x16x32_bf16 v[32:35], a[198:201], v[84:87], v[32:35]\n"
    "buffer_load_dwordx4 v[208:211], v25, s[40:43], 0 offen offset:0 \n"
    "v_and_b32 v169, 0xffff0000, v212\n"
    "v_mfma_f32_16x16x32_bf16 v[36:39], a[206:209], v[84:87], v[36:39]\n"
    "v_lshlrev_b32 v170, 16, v213\n"
    "v_and_b32 v171, 0xffff0000, v213\n"
    "v_mfma_f32_16x16x32_bf16 v[40:43], a[214:217], v[84:87], v[40:43]\n"
    "ds_read_b128 v[84:87], v4 offset:6272\n"
    "s_waitcnt lgkmcnt(5)\n"
    "v_mfma_f32_16x16x32_bf16 v[160:163], a[220:223], v[100:103], v[160:163]\n"
    "ds_write_b128 v16, v[32:35] offset:0\n"
    "v_mfma_f32_16x16x32_bf16 v[164:167], a[224:227], v[100:103], v[164:167]\n"
    "s_waitcnt lgkmcnt(5)\n"
    "buffer_load_dwordx2 v[212:213], v26, s[40:43], 0 offen offset:64 \n"
    "v_mfma_f32_16x16x32_bf16 v[168:171], a[228:231], v[100:103], v[168:171]\n"
    "v_mfma_f32_16x16x32_bf16 v[160:163], a[232:235], v[104:107], v[160:163]\n"
    "ds_write_b128 v16, v[36:39] offset:1024\n"
    "v_mfma_f32_16x16x32_bf16 v[164:167], a[236:239], v[104:107], v[164:167]\n"
    "s_waitcnt lgkmcnt(4)\n"
    "v_mfma_f32_16x16x32_bf16 v[168:171], a[240:243], v[104:107], v[168:171]\n"
    "s_waitcnt lgkmcnt(3)\n"
    "v_mfma_f32_16x16x32_bf16 v[160:163], a[244:247], v[108:111], v[160:163]\n"
    "ds_write_b128 v16, v[40:43] offset:2048\n"
    "v_mfma_f32_16x16x32_bf16 v[164:167], a[248:251], v[108:111], v[164:167]\n"
    "v_mfma_f32_16x16x32_bf16 v[168:171], a[252:255], v[108:111], v[168:171]\n"
    "s_waitcnt lgkmcnt(3)\n"
    "s_nop 3\n"
    "v_sub_f32 v220, v172, v160\n"
    "v_sub_f32 v221, v173, v161\n"
    "v_mfma_f32_16x16x32_bf16 v[44:47], a[2:5], v[88:91], v[32:35]\n"
    "v_cvt_pk_bf16_f32 v228, v220, v221\n"
    "v_sub_f32 v222, v174, v162\n"
    "v_mfma_f32_16x16x32_bf16 v[48:51], a[10:13], v[88:91], v[36:39]\n"
    "v_sub_f32 v223, v175, v163\n"
    "v_cvt_pk_bf16_f32 v229, v222, v223\n"
    "v_mfma_f32_16x16x32_bf16 v[52:55], a[18:21], v[88:91], v[40:43]\n"
    "ds_read_b128 v[88:91], v5 offset:6144\n"
    "s_waitcnt vmcnt(8) lgkmcnt(1)\n"
    "s_barrier\n"
    "v_mfma_f32_16x16x32_bf16 v[44:47], a[26:29], v[92:95], v[44:47]\n"
    "ds_read_b128 v[112:115], v6 offset:18432\n"
    "v_sub_f32 v224, v176, v164\n"
    "v_mfma_f32_16x16x32_bf16 v[48:51], a[34:37], v[92:95], v[48:51]\n"
    "ds_read_b128 v[116:119], v6 offset:18496\n"
    "v_sub_f32 v225, v177, v165\n"
    "v_mfma_f32_16x16x32_bf16 v[52:55], a[42:45], v[92:95], v[52:55]\n"
    "ds_read_b128 v[92:95], v5 offset:6208\n"
    "v_cvt_pk_bf16_f32 v230, v224, v225\n"
    "v_mfma_f32_16x16x32_bf16 v[44:47], a[50:53], v[96:99], v[44:47]\n"
    "ds_read_b128 v[120:123], v6 offset:18560\n"
    "v_sub_f32 v226, v178, v166\n"
    "v_mfma_f32_16x16x32_bf16 v[48:51], a[58:61], v[96:99], v[48:51]\n"
    "ds_read_b128 v[56:59], v17 offset:0\n"
    "v_sub_f32 v227, v179, v167\n"
    "v_mfma_f32_16x16x32_bf16 v[52:55], a[66:69], v[96:99], v[52:55]\n"
    "ds_read_b128 v[96:99], v5 offset:6272\n"
    "v_cvt_pk_bf16_f32 v231, v226, v227\n"
    "v_mfma_f32_16x16x32_bf16 v[44:47], a[74:77], v[76:79], v[44:47]\n"
    "ds_read_b128 v[60:63], v17 offset:1024\n"
    "v_sub_f32 v220, v180, v168\n"
    "v_mfma_f32_16x16x32_bf16 v[48:51], a[82:85], v[76:79], v[48:51]\n"
    "ds_read_b128 v[64:67], v17 offset:2048\n"
    "v_sub_f32 v221, v181, v169\n"
    "v_mfma_f32_16x16x32_bf16 v[52:55], a[90:93], v[76:79], v[52:55]\n"
    "ds_read_b128 v[76:79], v0 offset:38400\n"
    "v_cvt_pk_bf16_f32 v232, v220, v221\n"
    "v_mfma_f32_16x16x32_bf16 v[44:47], a[98:101], v[80:83], v[44:47]\n"
    "ds_read_b128 v[252:255], v248 offset:6144\n"
    "v_sub_f32 v222, v182, v170\n"
    "v_mfma_f32_16x16x32_bf16 v[48:51], a[106:109], v[80:83], v[48:51]\n"
    "ds_read_b128 v[240:243], v249 offset:6144\n"
    "v_sub_f32 v223, v183, v171\n"
    "v_mfma_f32_16x16x32_bf16 v[52:55], a[114:117], v[80:83], v[52:55]\n"
    "ds_read_b128 v[80:83], v0 offset:38464\n"
    "v_cvt_pk_bf16_f32 v233, v222, v223\n"
    "v_mfma_f32_16x16x32_bf16 v[44:47], a[122:125], v[84:87], v[44:47]\n"
    "s_add_u32 m0, s45, 0x1bc00\n"
    "s_nop 0\n"
    "buffer_load_dwordx4 v18, s[36:39], 0 offen nt lds\n"
    "v_mfma_f32_16x16x32_bf16 v[48:51], a[130:133], v[84:87], v[48:51]\n"
    "ds_read_b128 v[100:103], v6 offset:27648\n"
    "v_add_f32_dpp v224, v172, v160 row_ror:8 row_mask:0xf bank_mask:0x3\n"
    "v_mfma_f32_16x16x32_bf16 v[52:55], a[138:141], v[84:87], v[52:55]\n"
    "s_waitcnt lgkmcnt(9)\n"
    "ds_read_b128 v[84:87], v0 offset:38528\n"
    "v_mfma_f32_16x16x32_bf16 v[44:47], a[146:149], v[88:91], v[44:47]\n"
    "ds_read_b128 v[104:107], v6 offset:27712\n"
    "v_sub_f32_dpp v224, v160, v172 row_ror:8 row_mask:0xf bank_mask:0xc\n"
    "v_mfma_f32_16x16x32_bf16 v[48:51], a[154:157], v[88:91], v[48:51]\n"
    "ds_read_b128 v[108:111], v6 offset:27776\n"
    "v_add_f32_dpp v225, v173, v161 row_ror:8 row_mask:0xf bank_mask:0x3\n"
    "v_mfma_f32_16x16x32_bf16 v[52:55], a[162:165], v[88:91], v[52:55]\n"
    "ds_read_b128 v[88:91], v1 offset:38400\n"
    "v_sub_f32_dpp v225, v161, v173 row_ror:8 row_mask:0xf bank_mask:0xc\n"
    "v_mfma_f32_16x16x32_bf16 v[44:47], a[170:173], v[92:95], v[44:47]\n"
    "s_waitcnt lgkmcnt(7)\n"
    "s_add_u32 s55, s47, 0x48c00\n"
    "buffer_store_dwordx4 v[252:255], v31, s[24:27], s55 offen offset:0 sc1 nt\n"
    "v_mfma_f32_16x16x32_bf16 v[48:51], a[178:181], v[92:95], v[48:51]\n"
    "ds_write_b128 v12, v[228:231] offset:38400\n"
    "v_mfma_f32_16x16x32_bf16 v[52:55], a[186:189], v[92:95], v[52:55]\n"
    "s_waitcnt lgkmcnt(6)\n"
    "ds_read_b128 v[92:95], v1 offset:38464\n"
    "v_mfma_f32_16x16x32_bf16 v[44:47], a[194:197], v[96:99], v[44:47]\n"
    "v_cvt_pk_bf16_f32 v234, v224, v225\n"
    "v_add_f32_dpp v226, v174, v162 row_ror:8 row_mask:0xf bank_mask:0x3\n"
    "v_mfma_f32_16x16x32_bf16 v[48:51], a[202:205], v[96:99], v[48:51]\n"
    "s_add_u32 s52, s47, 0x48c00\n"
    "s_mov_b32 exec_hi, 0\n"
    "buffer_store_dwordx4 v[240:243], v31, s[24:27], s52 offen offset:1024 sc1 nt\n"
    "s_mov_b32 exec_hi, -1\n"
    "v_mfma_f32_16x16x32_bf16 v[52:55], a[210:213], v[96:99], v[52:55]\n"
    "ds_read_b128 v[96:99], v1 offset:38528\n"
    "v_fma_f32 v56, v44, s44, v56\n"
    "v_mfma_f32_16x16x32_bf16 v[32:35], a[6:9], v[76:79], 0\n"
    "ds_write_b64 v13, v[232:233] offset:38400\n"
    "v_mfma_f32_16x16x32_bf16 v[36:39], a[14:17], v[76:79], 0\n"
    "s_waitcnt lgkmcnt(4)\n"
    "v_fma_f32 v57, v45, s44, v57\n"
    "v_cvt_pk_bf16_f32 v68, v56, v57\n"
    "v_mfma_f32_16x16x32_bf16 v[40:43], a[22:25], v[76:79], 0\n"
    "ds_read_b128 v[76:79], v2 offset:38400\n"
    "v_fma_f32 v58, v46, s44, v58\n"
    "v_mfma_f32_16x16x32_bf16 v[32:35], a[30:33], v[80:83], v[32:35]\n"
    "s_add_u32 m0, s45, 0x1cc00\n"
    "s_nop 0\n"
    "buffer_load_dwordx4 v19, s[36:39], 0 offen nt lds\n"
    "v_mfma_f32_16x16x32_bf16 v[36:39], a[38:41], v[80:83], v[36:39]\n"
    "v_fma_f32 v59, v47, s44, v59\n"
    "v_cvt_pk_bf16_f32 v69, v58, v59\n"
    "v_mfma_f32_16x16x32_bf16 v[40:43], a[46:49], v[80:83], v[40:43]\n"
    "ds_read_b128 v[80:83], v2 offset:38464\n"
    "v_fma_f32 v60, v48, s44, v60\n"
    "v_mfma_f32_16x16x32_bf16 v[32:35], a[54:57], v[84:87], v[32:35]\n"
    "s_waitcnt lgkmcnt(4)\n"
    "v_fma_f32 v61, v49, s44, v61\n"
    "v_cvt_pk_bf16_f32 v70, v60, v61\n"
    "v_mfma_f32_16x16x32_bf16 v[36:39], a[62:65], v[84:87], v[36:39]\n"
    "v_fma_f32 v62, v50, s44, v62\n"
    "v_fma_f32 v63, v51, s44, v63\n"
    "v_mfma_f32_16x16x32_bf16 v[40:43], a[70:73], v[84:87], v[40:43]\n"
    "ds_read_b128 v[84:87], v2 offset:38528\n"
    "v_cvt_pk_bf16_f32 v71, v62, v63\n"
    "v_mfma_f32_16x16x32_bf16 v[32:35], a[78:81], v[88:91], v[32:35]\n"
    "ds_write_b128 v27, v[68:71] offset:0\n"
    "v_mfma_f32_16x16x32_bf16 v[36:39], a[86:89], v[88:91], v[36:39]\n"
    "s_add_u32 m0, s45, 0x1dc00\n"
    "s_nop 0\n"
    "buffer_load_dwordx4 v20, s[36:39], 0 offen nt lds\n"
    "v_mfma_f32_16x16x32_bf16 v[40:43], a[94:97], v[88:91], v[40:43]\n"
    "ds_read_b128 v[88:91], v3 offset:38400\n"
    "v_fma_f32 v64, v52, s44, v64\n"
    "v_mfma_f32_16x16x32_bf16 v[32:35], a[102:105], v[92:95], v[32:35]\n"
    "s_waitcnt lgkmcnt(4)\n"
    "v_fma_f32 v65, v53, s44, v65\n"
    "v_cvt_pk_bf16_f32 v72, v64, v65\n"
    "v_mfma_f32_16x16x32_bf16 v[36:39], a[110:113], v[92:95], v[36:39]\n"
    "v_fma_f32 v66, v54, s44, v66\n"
    "v_fma_f32 v67, v55, s44, v67\n"
    "v_mfma_f32_16x16x32_bf16 v[40:43], a[118:121], v[92:95], v[40:43]\n"
    "ds_read_b128 v[92:95], v3 offset:38464\n"
    "v_cvt_pk_bf16_f32 v73, v66, v67\n"
    "v_mfma_f32_16x16x32_bf16 v[32:35], a[126:129], v[96:99], v[32:35]\n"
    "ds_write_b64 v28, v[72:73] offset:0\n"
    "v_mfma_f32_16x16x32_bf16 v[36:39], a[134:137], v[96:99], v[36:39]\n"
    "s_add_u32 m0, s45, 0x1ec00\n"
    "s_nop 0\n"
    "buffer_load_dwordx4 v21, s[36:39], 0 offen nt lds\n"
    "v_mfma_f32_16x16x32_bf16 v[40:43], a[142:145], v[96:99], v[40:43]\n"
    "ds_read_b128 v[96:99], v3 offset:38528\n"
    "v_sub_f32_dpp v226, v162, v174 row_ror:8 row_mask:0xf bank_mask:0xc\n"
    "v_mfma_f32_16x16x32_bf16 v[32:35], a[150:153], v[76:79], v[32:35]\n"
    "s_waitcnt lgkmcnt(5)\n"
    "v_add_f32_dpp v227, v175, v163 row_ror:8 row_mask:0xf bank_mask:0x3\n"
    "v_sub_f32_dpp v227, v163, v175 row_ror:8 row_mask:0xf bank_mask:0xc\n"
    "v_mfma_f32_16x16x32_bf16 v[36:39], a[158:161], v[76:79], v[36:39]\n"
    "v_cvt_pk_bf16_f32 v235, v226, v227\n"
    "v_add_f32_dpp v220, v176, v164 row_ror:8 row_mask:0xf bank_mask:0x3\n"
    "v_mfma_f32_16x16x32_bf16 v[40:43], a[166:169], v[76:79], v[40:43]\n"
    "ds_read_b128 v[76:79], v4 offset:38400\n"
    "v_sub_f32_dpp v220, v164, v176 row_ror:8 row_mask:0xf bank_mask:0xc\n"
    "v_mfma_f32_16x16x32_bf16 v[32:35], a[174:177], v[80:83], v[32:35]\n"
    "s_add_u32 m0, s46, 0x1fc00\n"
    "s_mov_b32 exec_hi, 0\n"
    "buffer_load_dwordx4 v22, s[36:39], 0 offen nt lds\n"
    "s_mov_b32 exec_hi, -1\n"
    "v_mfma_f32_16x16x32_bf16 v[36:39], a[182:185], v[80:83], v[36:39]\n"
    "s_waitcnt lgkmcnt(4)\n"
    "v_add_f32_dpp v221, v177, v165 row_ror:8 row_mask:0xf bank_mask:0x3\n"
    "v_sub_f32_dpp v221, v165, v177 row_ror:8 row_mask:0xf bank_mask:0xc\n"
    "v_mfma_f32_16x16x32_bf16 v[40:43], a[190:193], v[80:83], v[40:43]\n"
    "ds_read_b128 v[80:83], v4 offset:38464\n"
    "v_cvt_pk_bf16_f32 v236, v220, v221\n"
    "v_mfma_f32_16x16x32_bf16 v[32:35], a[198:201], v[84:87], v[32:35]\n"
    "s_waitcnt lgkmcnt(4)\n"
    "v_add_f32_dpp v222, v178, v166 row_ror:8 row_mask:0xf bank_mask:0x3\n"
    "v_sub_f32_dpp v222, v166, v178 row_ror:8 row_mask:0xf bank_mask:0xc\n"
    "v_mfma_f32_16x16x32_bf16 v[36:39], a[206:209], v[84:87], v[36:39]\n"
    "v_add_f32_dpp v223, v179, v167 row_ror:8 row_mask:0xf bank_mask:0x3\n"
    "v_sub_f32_dpp v223, v167, v179 row_ror:8 row_mask:0xf bank_mask:0xc\n"
    "v_mfma_f32_16x16x32_bf16 v[40:43], a[214:217], v[84:87], v[40:43]\n"
    "ds_read_b128 v[84:87], v4 offset:38528\n"
    "v_cvt_pk_bf16_f32 v237, v222, v223\n"
    "v_mfma_f32_16x16x32_bf16 v[136:139], a[220:223], v[112:115], v[184:187]\n"
    "ds_write_b128 v14, v[234:237] offset:38400\n"
    "v_mfma_f32_16x16x32_bf16 v[140:143], a[224:227], v[112:115], v[188:191]\n"
    "s_waitcnt lgkmcnt(4)\n"
    "v_add_f32_dpp v224, v180, v168 row_ror:8 row_mask:0xf bank_mask:0x3\n"
    "v_sub_f32_dpp v224, v168, v180 row_ror:8 row_mask:0xf bank_mask:0xc\n"
    "v_mfma_f32_16x16x32_bf16 v[144:147], a[228:231], v[112:115], v[192:195]\n"
    "ds_read_b128 v[112:115], v7 offset:18432\n"
    "v_add_f32_dpp v225, v181, v169 row_ror:8 row_mask:0xf bank_mask:0x3\n"
    "v_mfma_f32_16x16x32_bf16 v[136:139], a[232:235], v[116:119], v[136:139]\n"
    "ds_write_b128 v16, v[32:35] offset:12288\n"
    "v_mfma_f32_16x16x32_bf16 v[140:143], a[236:239], v[116:119], v[140:143]\n"
    "s_waitcnt lgkmcnt(5)\n"
    "v_sub_f32_dpp v225, v169, v181 row_ror:8 row_mask:0xf bank_mask:0xc\n"
    "v_cvt_pk_bf16_f32 v238, v224, v225\n"
    "v_mfma_f32_16x16x32_bf16 v[144:147], a[240:243], v[116:119], v[144:147]\n"
    "ds_read_b128 v[116:119], v7 offset:18496\n"
    "v_add_f32_dpp v226, v182, v170 row_ror:8 row_mask:0xf bank_mask:0x3\n"
    "v_mfma_f32_16x16x32_bf16 v[136:139], a[244:247], v[120:123], v[136:139]\n"
    "ds_write_b128 v16, v[36:39] offset:13312\n"
    "v_mfma_f32_16x16x32_bf16 v[140:143], a[248:251], v[120:123], v[140:143]\n"
    "s_waitcnt lgkmcnt(6)\n"
    "v_sub_f32_dpp v226, v170, v182 row_ror:8 row_mask:0xf bank_mask:0xc\n"
    "v_add_f32_dpp v227, v183, v171 row_ror:8 row_mask:0xf bank_mask:0x3\n"
    "v_mfma_f32_16x16x32_bf16 v[144:147], a[252:255], v[120:123], v[144:147]\n"
    "ds_read_b128 v[120:123], v7 offset:18560\n"
    "v_sub_f32_dpp v227, v171, v183 row_ror:8 row_mask:0xf bank_mask:0xc\n"
    "v_mfma_f32_16x16x32_bf16 v[44:47], a[2:5], v[88:91], v[32:35]\n"
    "ds_write_b128 v16, v[40:43] offset:14336\n"
    "v_mfma_f32_16x16x32_bf16 v[48:51], a[10:13], v[88:91], v[36:39]\n"
    "s_waitcnt lgkmcnt(7)\n"
    "v_cvt_pk_bf16_f32 v239, v226, v227\n"
    "v_lshlrev_b32 v184, 16, v196\n"
    "v_mfma_f32_16x16x32_bf16 v[52:55], a[18:21], v[88:91], v[40:43]\n"
    "ds_read_b128 v[88:91], v5 offset:38400\n"
    "v_and_b32 v185, 0xffff0000, v196\n"
    "v_mfma_f32_16x16x32_bf16 v[44:47], a[26:29], v[92:95], v[44:47]\n"
    "ds_write_b64 v15, v[238:239] offset:38400\n"
    "v_mfma_f32_16x16x32_bf16 v[48:51], a[34:37], v[92:95], v[48:51]\n"
    "s_waitcnt lgkmcnt(7)\n"
    "v_lshlrev_b32 v186, 16, v197\n"
    "v_and_b32 v187, 0xffff0000, v197\n"
    "v_mfma_f32_16x16x32_bf16 v[52:55], a[42:45], v[92:95], v[52:55]\n"
    "ds_read_b128 v[92:95], v5 offset:38464\n"
    "s_waitcnt lgkmcnt(3)\n"
    "s_barrier\n"
    "v_mfma_f32_16x16x32_bf16 v[44:47], a[50:53], v[96:99], v[44:47]\n"
    "ds_read_b128 v[56:59], v17 offset:12288\n"
    "v_lshlrev_b32 v188, 16, v198\n"
    "v_mfma_f32_16x16x32_bf16 v[48:51], a[58:61], v[96:99], v[48:51]\n"
    "ds_read_b128 v[60:63], v17 offset:13312\n"
    "v_and_b32 v189, 0xffff0000, v198\n"
    "v_mfma_f32_16x16x32_bf16 v[52:55], a[66:69], v[96:99], v[52:55]\n"
    "ds_read_b128 v[96:99], v5 offset:38528\n"
    "v_lshlrev_b32 v190, 16, v199\n"
    "v_mfma_f32_16x16x32_bf16 v[44:47], a[74:77], v[76:79], v[44:47]\n"
    "ds_read_b128 v[64:67], v17 offset:14336\n"
    "v_and_b32 v191, 0xffff0000, v199\n"
    "v_mfma_f32_16x16x32_bf16 v[48:51], a[82:85], v[76:79], v[48:51]\n"
    "ds_read_b128 v[252:255], v248 offset:0\n"
    "v_lshlrev_b32 v192, 16, v200\n"
    "v_mfma_f32_16x16x32_bf16 v[52:55], a[90:93], v[76:79], v[52:55]\n"
    "ds_read_b128 v[76:79], v0 offset:41472\n"
    "v_and_b32 v193, 0xffff0000, v200\n"
    "v_mfma_f32_16x16x32_bf16 v[44:47], a[98:101], v[80:83], v[44:47]\n"
    "ds_read_b128 v[240:243], v249 offset:0\n"
    "v_lshlrev_b32 v194, 16, v201\n"
    "v_mfma_f32_16x16x32_bf16 v[48:51], a[106:109], v[80:83], v[48:51]\n"
    "s_waitcnt lgkmcnt(9)\n"
    "v_and_b32 v195, 0xffff0000, v201\n"
    "v_sub_f32 v220, v148, v136\n"
    "v_mfma_f32_16x16x32_bf16 v[52:55], a[114:117], v[80:83], v[52:55]\n"
    "ds_read_b128 v[80:83], v0 offset:41536\n"
    "v_sub_f32 v221, v149, v137\n"
    "v_mfma_f32_16x16x32_bf16 v[44:47], a[122:125], v[84:87], v[44:47]\n"
    "v_cvt_pk_bf16_f32 v228, v220, v221\n"
    "v_sub_f32 v222, v150, v138\n"
    "v_mfma_f32_16x16x32_bf16 v[48:51], a[130:133], v[84:87], v[48:51]\n"
    "s_waitcnt lgkmcnt(8)\n"
    "v_sub_f32 v223, v151, v139\n"
    "v_cvt_pk_bf16_f32 v229, v222, v223\n"
    "v_mfma_f32_16x16x32_bf16 v[52:55], a[138:141], v[84:87], v[52:55]\n"
    "ds_read_b128 v[84:87], v0 offset:41600\n"
    "v_sub_f32 v224, v152, v140\n"
    "v_mfma_f32_16x16x32_bf16 v[44:47], a[146:149], v[88:91], v[44:47]\n"
    "s_waitcnt lgkmcnt(7)\n"
    "v_sub_f32 v225, v153, v141\n"
    "v_cvt_pk_bf16_f32 v230, v224, v225\n"
    "v_mfma_f32_16x16x32_bf16 v[48:51], a[154:157], v[88:91], v[48:51]\n"
    "s_waitcnt lgkmcnt(4)\n"
    "s_add_u32 s53, s47, 0x49800\n"
    "buffer_store_dwordx4 v[252:255], v31, s[24:27], s53 offen offset:0 sc1 nt\n"
    "v_mfma_f32_16x16x32_bf16 v[52:55], a[162:165], v[88:91], v[52:55]\n"
    "ds_read_b128 v[88:91], v1 offset:41472\n"
    "v_sub_f32 v226, v154, v142\n"
    "v_mfma_f32_16x16x32_bf16 v[44:47], a[170:173], v[92:95], v[44:47]\n"
    "s_waitcnt lgkmcnt(4)\n"
    "v_sub_f32 v227, v155, v143\n"
    "v_cvt_pk_bf16_f32 v231, v226, v227\n"
    "v_mfma_f32_16x16x32_bf16 v[48:51], a[178:181], v[92:95], v[48:51]\n"
    "ds_write_b128 v8, v[228:231] offset:0\n"
    "v_mfma_f32_16x16x32_bf16 v[52:55], a[186:189], v[92:95], v[52:55]\n"
    "s_waitcnt lgkmcnt(4)\n"
    "s_add_u32 s54, s47, 0x49800\n"
    "s_mov_b32 exec_hi, 0\n"
    "buffer_store_dwordx4 v[240:243], v31, s[24:27], s54 offen offset:1024 sc1 nt\n"
    "s_mov_b32 exec_hi, -1\n"
    "v_mfma_f32_16x16x32_bf16 v[44:47], a[194:197], v[96:99], v[44:47]\n"
    "ds_read_b128 v[92:95], v1 offset:41536\n"
    "v_sub_f32 v220, v156, v144\n"
    "v_mfma_f32_16x16x32_bf16 v[48:51], a[202:205], v[96:99], v[48:51]\n"
    "ds_read_b128 v[240:243], v29 offset:4608\n"
    "v_sub_f32 v221, v157, v145\n"
    "v_mfma_f32_16x16x32_bf16 v[52:55], a[210:213], v[96:99], v[52:55]\n"
    "ds_read_b128 v[96:99], v1 offset:41600\n"
    "v_cvt_pk_bf16_f32 v232, v220, v221\n"
    "v_mfma_f32_16x16x32_bf16 v[32:35], a[6:9], v[76:79], 0\n"
    "s_waitcnt lgkmcnt(5)\n"
    "v_fma_f32 v56, v44, s44, v56\n"
    "v_fma_f32 v57, v45, s44, v57\n"
    "v_mfma_f32_16x16x32_bf16 v[36:39], a[14:17], v[76:79], 0\n"
    "v_cvt_pk_bf16_f32 v68, v56, v57\n"
    "v_fma_f32 v58, v46, s44, v58\n"
    "v_mfma_f32_16x16x32_bf16 v[40:43], a[22:25], v[76:79], 0\n"
    "ds_read_b128 v[76:79], v2 offset:41472\n"
    "v_fma_f32 v59, v47, s44, v59\n"
    "v_mfma_f32_16x16x32_bf16 v[32:35], a[30:33], v[80:83], v[32:35]\n"
    "s_waitcnt lgkmcnt(5)\n"
    "v_cvt_pk_bf16_f32 v69, v58, v59\n"
    "v_fma_f32 v60, v48, s44, v60\n"
    "v_mfma_f32_16x16x32_bf16 v[36:39], a[38:41], v[80:83], v[36:39]\n"
    "v_fma_f32 v61, v49, s44, v61\n"
    "v_cvt_pk_bf16_f32 v70, v60, v61\n"
    "v_mfma_f32_16x16x32_bf16 v[40:43], a[46:49], v[80:83], v[40:43]\n"
    "ds_read_b128 v[80:83], v2 offset:41536\n"
    "v_fma_f32 v62, v50, s44, v62\n"
    "v_mfma_f32_16x16x32_bf16 v[32:35], a[54:57], v[84:87], v[32:35]\n"
    "v_fma_f32 v63, v51, s44, v63\n"
    "v_cvt_pk_bf16_f32 v71, v62, v63\n"
    "v_mfma_f32_16x16x32_bf16 v[36:39], a[62:65], v[84:87], v[36:39]\n"
    "ds_write_b128 v27, v[68:71] offset:6144\n"
    "v_mfma_f32_16x16x32_bf16 v[40:43], a[70:73], v[84:87], v[40:43]\n"
    "s_waitcnt lgkmcnt(4)\n"
    "ds_read_b128 v[84:87], v2 offset:41600\n"
    "v_mfma_f32_16x16x32_bf16 v[32:35], a[78:81], v[88:91], v[32:35]\n"
    "s_waitcnt lgkmcnt(4)\n"
    "v_fma_f32 v64, v52, s44, v64\n"
    "v_fma_f32 v65, v53, s44, v65\n"
    "v_mfma_f32_16x16x32_bf16 v[36:39], a[86:89], v[88:91], v[36:39]\n"
    "ds_write_b128 v30, v[240:243] offset:0\n"
    "v_mfma_f32_16x16x32_bf16 v[40:43], a[94:97], v[88:91], v[40:43]\n"
    "ds_read_b128 v[88:91], v3 offset:41472\n"
    "v_mfma_f32_16x16x32_bf16 v[32:35], a[102:105], v[92:95], v[32:35]\n"
    "s_waitcnt lgkmcnt(5)\n"
    "v_cvt_pk_bf16_f32 v72, v64, v65\n"
    "v_fma_f32 v66, v54, s44, v66\n"
    "v_mfma_f32_16x16x32_bf16 v[36:39], a[110:113], v[92:95], v[36:39]\n"
    "v_fma_f32 v67, v55, s44, v67\n"
    "v_cvt_pk_bf16_f32 v73, v66, v67\n"
    "v_mfma_f32_16x16x32_bf16 v[40:43], a[118:121], v[92:95], v[40:43]\n"
    "ds_read_b128 v[92:95], v3 offset:41536\n"
    "v_sub_f32 v222, v158, v146\n"
    "v_mfma_f32_16x16x32_bf16 v[32:35], a[126:129], v[96:99], v[32:35]\n"
    "ds_write_b64 v28, v[72:73] offset:6144\n"
    "v_mfma_f32_16x16x32_bf16 v[36:39], a[134:137], v[96:99], v[36:39]\n"
    "s_waitcnt lgkmcnt(6)\n"
    "v_sub_f32 v223, v159, v147\n"
    "v_cvt_pk_bf16_f32 v233, v222, v223\n"
    "v_mfma_f32_16x16x32_bf16 v[40:43], a[142:145], v[96:99], v[40:43]\n"
    "ds_read_b128 v[96:99], v3 offset:41600\n"
    "v_add_f32 v224, v124, v136\n"
    "v_mfma_f32_16x16x32_bf16 v[32:35], a[150:153], v[76:79], v[32:35]\n"
    "ds_write_b64 v9, v[232:233] offset:0\n"
    "v_mfma_f32_16x16x32_bf16 v[36:39], a[158:161], v[76:79], v[36:39]\n"
    "s_waitcnt lgkmcnt(6)\n"
    "v_add_f32 v225, v125, v137\n"
    "v_cvt_pk_bf16_f32 v234, v224, v225\n"
    "v_mfma_f32_16x16x32_bf16 v[40:43], a[166:169], v[76:79], v[40:43]\n"
    "ds_read_b128 v[76:79], v4 offset:41472\n"
    "v_add_f32 v226, v126, v138\n"
    "v_mfma_f32_16x16x32_bf16 v[32:35], a[174:177], v[80:83], v[32:35]\n"
    "v_add_f32 v227, v127, v139\n"
    "v_cvt_pk_bf16_f32 v235, v226, v227\n"
    "v_mfma_f32_16x16x32_bf16 v[36:39], a[182:185], v[80:83], v[36:39]\n"
    "s_waitcnt lgkmcnt(5)\n"
    "v_add_f32 v220, v128, v140\n"
    "v_add_f32 v221, v129, v141\n"
    "v_mfma_f32_16x16x32_bf16 v[40:43], a[190:193], v[80:83], v[40:43]\n"
    "ds_read_b128 v[80:83], v4 offset:41536\n"
    "v_cvt_pk_bf16_f32 v236, v220, v221\n"
    "v_mfma_f32_16x16x32_bf16 v[32:35], a[198:201], v[84:87], v[32:35]\n"
    "v_add_f32 v222, v130, v142\n"
    "v_add_f32 v223, v131, v143\n"
    "v_mfma_f32_16x16x32_bf16 v[36:39], a[206:209], v[84:87], v[36:39]\n"
    "s_waitcnt lgkmcnt(5)\n"
    "v_cvt_pk_bf16_f32 v237, v222, v223\n"
    "v_add_f32 v224, v132, v144\n"
    "v_mfma_f32_16x16x32_bf16 v[40:43], a[214:217], v[84:87], v[40:43]\n"
    "ds_read_b128 v[84:87], v4 offset:41600\n"
    "v_add_f32 v225, v133, v145\n"
    "v_mfma_f32_16x16x32_bf16 v[148:151], a[220:223], v[100:103], v[184:187]\n"
    "ds_write_b128 v8, v[234:237] offset:9600\n"
    "v_mfma_f32_16x16x32_bf16 v[152:155], a[224:227], v[100:103], v[188:191]\n"
    "s_waitcnt lgkmcnt(5)\n"
    "v_cvt_pk_bf16_f32 v238, v224, v225\n"
    "v_add_f32 v226, v134, v146\n"
    "v_mfma_f32_16x16x32_bf16 v[156:159], a[228:231], v[100:103], v[192:195]\n"
    "v_add_f32 v227, v135, v147\n"
    "v_cvt_pk_bf16_f32 v239, v226, v227\n"
    "v_mfma_f32_16x16x32_bf16 v[148:151], a[232:235], v[104:107], v[148:151]\n"
    "ds_write_b64 v9, v[238:239] offset:9600\n"
    "v_mfma_f32_16x16x32_bf16 v[152:155], a[236:239], v[104:107], v[152:155]\n"
    "s_waitcnt lgkmcnt(4)\n"
    "v_sub_f32 v220, v136, v124\n"
    "v_sub_f32 v221, v137, v125\n"
    "v_mfma_f32_16x16x32_bf16 v[156:159], a[240:243], v[104:107], v[156:159]\n"
    "v_cvt_pk_bf16_f32 v228, v220, v221\n"
    "v_sub_f32 v222, v138, v126\n"
    "v_mfma_f32_16x16x32_bf16 v[148:151], a[244:247], v[108:111], v[148:151]\n"
    "ds_write_b128 v16, v[32:35] offset:0\n"
    "v_mfma_f32_16x16x32_bf16 v[152:155], a[248:251], v[108:111], v[152:155]\n"
    "s_waitcnt lgkmcnt(4)\n"
    "v_sub_f32 v223, v139, v127\n"
    "v_cvt_pk_bf16_f32 v229, v222, v223\n"
    "v_mfma_f32_16x16x32_bf16 v[156:159], a[252:255], v[108:111], v[156:159]\n"
    "s_waitcnt lgkmcnt(3)\n"
    "v_sub_f32 v224, v140, v128\n"
    "v_sub_f32 v225, v141, v129\n"
    "v_mfma_f32_16x16x32_bf16 v[44:47], a[2:5], v[88:91], v[32:35]\n"
    "ds_write_b128 v16, v[36:39] offset:1024\n"
    "v_mfma_f32_16x16x32_bf16 v[48:51], a[10:13], v[88:91], v[36:39]\n"
    "v_cvt_pk_bf16_f32 v230, v224, v225\n"
    "v_sub_f32 v226, v142, v130\n"
    "v_mfma_f32_16x16x32_bf16 v[52:55], a[18:21], v[88:91], v[40:43]\n"
    "ds_read_b128 v[88:91], v5 offset:41472\n"
    "v_sub_f32 v227, v143, v131\n"
    "v_mfma_f32_16x16x32_bf16 v[44:47], a[26:29], v[92:95], v[44:47]\n"
    "ds_write_b128 v16, v[40:43] offset:2048\n"
    "v_mfma_f32_16x16x32_bf16 v[48:51], a[34:37], v[92:95], v[48:51]\n"
    "v_cvt_pk_bf16_f32 v231, v226, v227\n"
    "v_sub_f32 v220, v144, v132\n"
    "v_mfma_f32_16x16x32_bf16 v[52:55], a[42:45], v[92:95], v[52:55]\n"
    "ds_read_b128 v[92:95], v5 offset:41536\n"
    "v_sub_f32 v221, v145, v133\n"
    "v_mfma_f32_16x16x32_bf16 v[44:47], a[50:53], v[96:99], v[44:47]\n"
    "ds_write_b128 v8, v[228:231] offset:19200\n"
    "v_mfma_f32_16x16x32_bf16 v[48:51], a[58:61], v[96:99], v[48:51]\n"
    "v_cvt_pk_bf16_f32 v232, v220, v221\n"
    "v_sub_f32 v222, v146, v134\n"
    "v_mfma_f32_16x16x32_bf16 v[52:55], a[66:69], v[96:99], v[52:55]\n"
    "ds_read_b128 v[96:99], v5 offset:41600\n"
    "s_waitcnt lgkmcnt(3)\n"
    "s_barrier\n"
    "v_mfma_f32_16x16x32_bf16 v[44:47], a[74:77], v[76:79], v[44:47]\n"
    "ds_read_b128 v[56:59], v17 offset:0\n"
    "v_sub_f32 v223, v147, v135\n"
    "v_mfma_f32_16x16x32_bf16 v[48:51], a[82:85], v[76:79], v[48:51]\n"
    "ds_read_b128 v[60:63], v17 offset:1024\n"
    "v_cvt_pk_bf16_f32 v233, v222, v223\n"
    "v_mfma_f32_16x16x32_bf16 v[52:55], a[90:93], v[76:79], v[52:55]\n"
    "ds_read_b128 v[76:79], v0 offset:44544\n"
    "v_sub_f32 v224, v124, v148\n"
    "v_mfma_f32_16x16x32_bf16 v[44:47], a[98:101], v[80:83], v[44:47]\n"
    "ds_read_b128 v[64:67], v17 offset:2048\n"
    "v_sub_f32 v225, v125, v149\n"
    "v_mfma_f32_16x16x32_bf16 v[48:51], a[106:109], v[80:83], v[48:51]\n"
    "ds_read_b128 v[252:255], v248 offset:6144\n"
    "v_cvt_pk_bf16_f32 v234, v224, v225\n"
    "v_mfma_f32_16x16x32_bf16 v[52:55], a[114:117], v[80:83], v[52:55]\n"
    "ds_read_b128 v[80:83], v0 offset:44608\n"
    "v_sub_f32 v226, v126, v150\n"
    "v_mfma_f32_16x16x32_bf16 v[44:47], a[122:125], v[84:87], v[44:47]\n"
    "ds_read_b128 v[240:243], v249 offset:6144\n"
    "v_sub_f32 v227, v127, v151\n"
    "v_mfma_f32_16x16x32_bf16 v[48:51], a[130:133], v[84:87], v[48:51]\n"
    "ds_write_b64 v9, v[232:233] offset:19200\n"
    "v_mfma_f32_16x16x32_bf16 v[52:55], a[138:141], v[84:87], v[52:55]\n"
    "s_waitcnt lgkmcnt(10)\n"
    "v_cvt_pk_bf16_f32 v235, v226, v227\n"
    "v_sub_f32 v220, v128, v152\n"
    "v_mfma_f32_16x16x32_bf16 v[44:47], a[146:149], v[88:91], v[44:47]\n"
    "ds_read_b128 v[84:87], v0 offset:44672\n"
    "v_sub_f32 v221, v129, v153\n"
    "v_mfma_f32_16x16x32_bf16 v[48:51], a[154:157], v[88:91], v[48:51]\n"
    "s_waitcnt lgkmcnt(9)\n"
    "v_cvt_pk_bf16_f32 v236, v220, v221\n"
    "v_sub_f32 v222, v130, v154\n"
    "v_mfma_f32_16x16x32_bf16 v[52:55], a[162:165], v[88:91], v[52:55]\n"
    "ds_read_b128 v[88:91], v1 offset:44544\n"
    "v_sub_f32 v223, v131, v155\n"
    "v_mfma_f32_16x16x32_bf16 v[44:47], a[170:173], v[92:95], v[44:47]\n"
    "s_waitcnt lgkmcnt(8)\n"
    "v_cvt_pk_bf16_f32 v237, v222, v223\n"
    "v_sub_f32 v224, v132, v156\n"
    "v_mfma_f32_16x16x32_bf16 v[48:51], a[178:181], v[92:95], v[48:51]\n"
    "s_waitcnt lgkmcnt(5)\n"
    "s_add_u32 s55, s47, 0x6c000\n"
    "buffer_store_dwordx4 v[252:255], v31, s[24:27], s55 offen offset:0 sc1 nt\n"
    "v_mfma_f32_16x16x32_bf16 v[52:55], a[186:189], v[92:95], v[52:55]\n"
    "ds_read_b128 v[92:95], v1 offset:44608\n"
    "v_sub_f32 v225, v133, v157\n"
    "v_mfma_f32_16x16x32_bf16 v[44:47], a[194:197], v[96:99], v[44:47]\n"
    "ds_write_b128 v8, v[234:237] offset:28800\n"
    "v_mfma_f32_16x16x32_bf16 v[48:51], a[202:205], v[96:99], v[48:51]\n"
    "s_waitcnt lgkmcnt(5)\n"
    "v_cvt_pk_bf16_f32 v238, v224, v225\n"
    "v_sub_f32 v226, v134, v158\n"
    "v_mfma_f32_16x16x32_bf16 v[52:55], a[210:213], v[96:99], v[52:55]\n"
    "ds_read_b128 v[96:99], v1 offset:44672\n"
    "s_nop 0\n"
    "v_fma_f32 v56, v44, s44, v56\n"
    "v_mfma_f32_16x16x32_bf16 v[32:35], a[6:9], v[76:79], 0\n"
    "s_add_u32 s52, s47, 0x6c000\n"
    "s_mov_b32 exec_hi, 0\n"
    "buffer_store_dwordx4 v[240:243], v31, s[24:27], s52 offen offset:1024 sc1 nt\n"
    "s_mov_b32 exec_hi, -1\n"
    "v_mfma_f32_16x16x32_bf16 v[36:39], a[14:17], v[76:79], 0\n"
    "s_waitcnt lgkmcnt(4)\n"
    "v_fma_f32 v57, v45, s44, v57\n"
    "v_cvt_pk_bf16_f32 v68, v56, v57\n"
    "v_mfma_f32_16x16x32_bf16 v[40:43], a[22:25], v[76:79], 0\n"
    "ds_read_b128 v[76:79], v2 offset:44544\n"
    "v_fma_f32 v58, v46, s44, v58\n"
    "v_mfma_f32_16x16x32_bf16 v[32:35], a[30:33], v[80:83], v[32:35]\n"
    "s_waitcnt lgkmcnt(4)\n"
    "v_fma_f32 v59, v47, s44, v59\n"
    "v_cvt_pk_bf16_f32 v69, v58, v59\n"
    "v_mfma_f32_16x16x32_bf16 v[36:39], a[38:41], v[80:83], v[36:39]\n"
    "v_fma_f32 v60, v48, s44, v60\n"
    "v_fma_f32 v61, v49, s44, v61\n"
    "v_mfma_f32_16x16x32_bf16 v[40:43], a[46:49], v[80:83], v[40:43]\n"
    "ds_read_b128 v[80:83], v2 offset:44608\n"
    "v_cvt_pk_bf16_f32 v70, v60, v61\n"
    "v_mfma_f32_16x16x32_bf16 v[32:35], a[54:57], v[84:87], v[32:35]\n"
    "s_waitcnt lgkmcnt(4)\n"
    "v_fma_f32 v62, v50, s44, v62\n"
    "v_fma_f32 v63, v51, s44, v63\n"
    "v_mfma_f32_16x16x32_bf16 v[36:39], a[62:65], v[84:87], v[36:39]\n"
    "v_cvt_pk_bf16_f32 v71, v62, v63\n"
    "v_fma_f32 v64, v52, s44, v64\n"
    "v_mfma_f32_16x16x32_bf16 v[40:43], a[70:73], v[84:87], v[40:43]\n"
    "ds_read_b128 v[84:87], v2 offset:44672\n"
    "v_fma_f32 v65, v53, s44, v65\n"
    "v_mfma_f32_16x16x32_bf16 v[32:35], a[78:81], v[88:91], v[32:35]\n"
    "ds_write_b128 v27, v[68:71] offset:0\n"
    "v_mfma_f32_16x16x32_bf16 v[36:39], a[86:89], v[88:91], v[36:39]\n"
    "s_waitcnt lgkmcnt(4)\n"
    "v_cvt_pk_bf16_f32 v72, v64, v65\n"
    "v_fma_f32 v66, v54, s44, v66\n"
    "v_mfma_f32_16x16x32_bf16 v[40:43], a[94:97], v[88:91], v[40:43]\n"
    "ds_read_b128 v[88:91], v3 offset:44544\n"
    "v_fma_f32 v67, v55, s44, v67\n"
    "v_mfma_f32_16x16x32_bf16 v[32:35], a[102:105], v[92:95], v[32:35]\n"
    "s_waitcnt lgkmcnt(4)\n"
    "v_cvt_pk_bf16_f32 v73, v66, v67\n"
    "v_sub_f32 v227, v135, v159\n"
    "v_mfma_f32_16x16x32_bf16 v[36:39], a[110:113], v[92:95], v[36:39]\n"
    "ds_write_b64 v28, v[72:73] offset:0\n"
    "v_mfma_f32_16x16x32_bf16 v[40:43], a[118:121], v[92:95], v[40:43]\n"
    "v_cvt_pk_bf16_f32 v239, v226, v227\n"
    "v_lshlrev_b32 v172, 16, v214\n"
    "v_mfma_f32_16x16x32_bf16 v[32:35], a[126:129], v[96:99], v[32:35]\n"
    "ds_read_b128 v[92:95], v3 offset:44608\n"
    "v_and_b32 v173, 0xffff0000, v214\n"
    "v_mfma_f32_16x16x32_bf16 v[36:39], a[134:137], v[96:99], v[36:39]\n"
    "ds_write_b64 v9, v[238:239] offset:28800\n"
    "v_mfma_f32_16x16x32_bf16 v[40:43], a[142:145], v[96:99], v[40:43]\n"
    "s_waitcnt lgkmcnt(6)\n"
    "v_lshlrev_b32 v174, 16, v215\n"
    "v_and_b32 v175, 0xffff0000, v215\n"
    "v_mfma_f32_16x16x32_bf16 v[32:35], a[150:153], v[76:79], v[32:35]\n"
    "ds_read_b128 v[96:99], v3 offset:44672\n"
    "v_lshlrev_b32 v176, 16, v216\n"
    "v_mfma_f32_16x16x32_bf16 v[36:39], a[158:161], v[76:79], v[36:39]\n"
    "s_waitcnt lgkmcnt(6)\n"
    "v_and_b32 v177, 0xffff0000, v216\n"
    "v_lshlrev_b32 v178, 16, v217\n"
    "v_mfma_f32_16x16x32_bf16 v[40:43], a[166:169], v[76:79], v[40:43]\n"
    "ds_read_b128 v[76:79], v4 offset:44544\n"
    "v_and_b32 v179, 0xffff0000, v217\n"
    "v_mfma_f32_16x16x32_bf16 v[32:35], a[174:177], v[80:83], v[32:35]\n"
    "v_lshlrev_b32 v180, 16, v218\n"
    "v_and_b32 v181, 0xffff0000, v218\n"
    "v_mfma_f32_16x16x32_bf16 v[36:39], a[182:185], v[80:83], v[36:39]\n"
    "s_waitcnt lgkmcnt(5)\n"
    "v_lshlrev_b32 v182, 16, v219\n"
    "v_and_b32 v183, 0xffff0000, v219\n"
    "v_mfma_f32_16x16x32_bf16 v[40:43], a[190:193], v[80:83], v[40:43]\n"
    "ds_read_b128 v[80:83], v4 offset:44608\n"
    "v_mfma_f32_16x16x32_bf16 v[32:35], a[198:201], v[84:87], v[32:35]\n"
    "v_mfma_f32_16x16x32_bf16 v[36:39], a[206:209], v[84:87], v[36:39]\n"
    "v_mfma_f32_16x16x32_bf16 v[40:43], a[214:217], v[84:87], v[40:43]\n"
    "ds_read_b128 v[84:87], v4 offset:44672\n"
    "s_waitcnt lgkmcnt(5)\n"
    "v_mfma_f32_16x16x32_bf16 v[172:175], a[220:223], v[112:115], v[172:175]\n"
    "s_nop 2\n"
    "ds_write_b128 v16, v[32:35] offset:12288\n"
    "v_mfma_f32_16x16x32_bf16 v[176:179], a[224:227], v[112:115], v[176:179]\n"
    "s_waitcnt lgkmcnt(4)\n"
    "v_mfma_f32_16x16x32_bf16 v[180:183], a[228:231], v[112:115], v[180:183]\n"
    "v_mfma_f32_16x16x32_bf16 v[172:175], a[232:235], v[116:119], v[172:175]\n"
    "ds_write_b128 v16, v[36:39] offset:13312\n"
    "v_mfma_f32_16x16x32_bf16 v[176:179], a[236:239], v[116:119], v[176:179]\n"
    "s_waitcnt lgkmcnt(4)\n"
    "v_mfma_f32_16x16x32_bf16 v[180:183], a[240:243], v[116:119], v[180:183]\n"
    "s_waitcnt lgkmcnt(3)\n"
    "v_mfma_f32_16x16x32_bf16 v[172:175], a[244:247], v[120:123], v[172:175]\n"
    "ds_write_b128 v16, v[40:43] offset:14336\n"
    "v_mfma_f32_16x16x32_bf16 v[176:179], a[248:251], v[120:123], v[176:179]\n"
    "v_mfma_f32_16x16x32_bf16 v[180:183], a[252:255], v[120:123], v[180:183]\n"
    "s_waitcnt lgkmcnt(3)\n"
    "s_nop 3\n"
    "v_sub_f32 v220, v160, v172\n"
    "v_sub_f32 v221, v161, v173\n"
    "v_mfma_f32_16x16x32_bf16 v[44:47], a[2:5], v[88:91], v[32:35]\n"
    "v_cvt_pk_bf16_f32 v228, v220, v221\n"
    "v_sub_f32 v222, v162, v174\n"
    "v_mfma_f32_16x16x32_bf16 v[48:51], a[10:13], v[88:91], v[36:39]\n"
    "v_sub_f32 v223, v163, v175\n"
    "v_cvt_pk_bf16_f32 v229, v222, v223\n"
    "v_mfma_f32_16x16x32_bf16 v[52:55], a[18:21], v[88:91], v[40:43]\n"
    "ds_read_b128 v[88:91], v5 offset:44544\n"
    "s_waitcnt vmcnt(4) lgkmcnt(1)\n"
    "s_barrier\n"
    "v_mfma_f32_16x16x32_bf16 v[44:47], a[26:29], v[92:95], v[44:47]\n"
    "ds_read_b128 v[100:103], v6 offset:0\n"
    "v_sub_f32 v224, v164, v176\n"
    "v_mfma_f32_16x16x32_bf16 v[48:51], a[34:37], v[92:95], v[48:51]\n"
    "ds_read_b128 v[104:107], v6 offset:64\n"
    "v_sub_f32 v225, v165, v177\n"
    "v_mfma_f32_16x16x32_bf16 v[52:55], a[42:45], v[92:95], v[52:55]\n"
    "ds_read_b128 v[92:95], v5 offset:44608\n"
    "v_cvt_pk_bf16_f32 v230, v224, v225\n"
    "v_mfma_f32_16x16x32_bf16 v[44:47], a[50:53], v[96:99], v[44:47]\n"
    "ds_read_b128 v[108:111], v6 offset:128\n"
    "v_sub_f32 v226, v166, v178\n"
    "v_mfma_f32_16x16x32_bf16 v[48:51], a[58:61], v[96:99], v[48:51]\n"
    "ds_read_b128 v[56:59], v17 offset:12288\n"
    "v_sub_f32 v227, v167, v179\n"
    "v_mfma_f32_16x16x32_bf16 v[52:55], a[66:69], v[96:99], v[52:55]\n"
    "ds_read_b128 v[96:99], v5 offset:44672\n"
    "v_cvt_pk_bf16_f32 v231, v226, v227\n"
    "v_mfma_f32_16x16x32_bf16 v[44:47], a[74:77], v[76:79], v[44:47]\n"
    "ds_read_b128 v[60:63], v17 offset:13312\n"
    "v_sub_f32 v220, v168, v180\n"
    "v_mfma_f32_16x16x32_bf16 v[48:51], a[82:85], v[76:79], v[48:51]\n"
    "ds_read_b128 v[64:67], v17 offset:14336\n"
    "v_sub_f32 v221, v169, v181\n"
    "v_mfma_f32_16x16x32_bf16 v[52:55], a[90:93], v[76:79], v[52:55]\n"
    "ds_read_b128 v[76:79], v0 offset:0\n"
    "v_cvt_pk_bf16_f32 v232, v220, v221\n"
    "v_mfma_f32_16x16x32_bf16 v[44:47], a[98:101], v[80:83], v[44:47]\n"
    "ds_read_b128 v[252:255], v248 offset:0\n"
    "v_sub_f32 v222, v170, v182\n"
    "v_mfma_f32_16x16x32_bf16 v[48:51], a[106:109], v[80:83], v[48:51]\n"
    "ds_read_b128 v[240:243], v249 offset:0\n"
    "v_sub_f32 v223, v171, v183\n"
    "v_mfma_f32_16x16x32_bf16 v[52:55], a[114:117], v[80:83], v[52:55]\n"
    "ds_read_b128 v[80:83], v0 offset:64\n"
    "v_cvt_pk_bf16_f32 v233, v222, v223\n"
    "v_mfma_f32_16x16x32_bf16 v[44:47], a[122:125], v[84:87], v[44:47]\n"
    "ds_read_b128 v[112:115], v6 offset:9216\n"
    "v_add_f32_dpp v224, v160, v172 row_ror:8 row_mask:0xf bank_mask:0x3\n"
    "v_mfma_f32_16x16x32_bf16 v[48:51], a[130:133], v[84:87], v[48:51]\n"
    "ds_read_b128 v[116:119], v6 offset:9280\n"
    "v_sub_f32_dpp v224, v172, v160 row_ror:8 row_mask:0xf bank_mask:0xc\n"
    "v_mfma_f32_16x16x32_bf16 v[52:55], a[138:141], v[84:87], v[52:55]\n"
    "s_waitcnt lgkmcnt(10)\n"
    "ds_read_b128 v[84:87], v0 offset:128\n"
    "v_mfma_f32_16x16x32_bf16 v[44:47], a[146:149], v[88:91], v[44:47]\n"
    "ds_read_b128 v[120:123], v6 offset:9344\n"
    "v_add_f32_dpp v225, v161, v173 row_ror:8 row_mask:0xf bank_mask:0x3\n"
    "v_mfma_f32_16x16x32_bf16 v[48:51], a[154:157], v[88:91], v[48:51]\n"
    "ds_write_b128 v12, v[228:231] offset:0\n"
    "v_mfma_f32_16x16x32_bf16 v[52:55], a[162:165], v[88:91], v[52:55]\n"
    "s_waitcnt lgkmcnt(7)\n"
    "s_add_u32 s53, s47, 0x6cc00\n"
    "buffer_store_dwordx4 v[252:255], v31, s[24:27], s53 offen offset:0 sc1 nt\n"
    "v_mfma_f32_16x16x32_bf16 v[44:47], a[170:173], v[92:95], v[44:47]\n"
    "ds_read_b128 v[88:91], v1 offset:0\n"
    "v_sub_f32_dpp v225, v173, v161 row_ror:8 row_mask:0xf bank_mask:0xc\n"
    "v_mfma_f32_16x16x32_bf16 v[48:51], a[178:181], v[92:95], v[48:51]\n"
    "ds_write_b64 v13, v[232:233] offset:0\n"
    "v_mfma_f32_16x16x32_bf16 v[52:55], a[186:189], v[92:95], v[52:55]\n"
    "s_waitcnt lgkmcnt(7)\n"
    "v_cvt_pk_bf16_f32 v234, v224, v225\n"
    "v_add_f32_dpp v226, v162, v174 row_ror:8 row_mask:0xf bank_mask:0x3\n"
    "v_mfma_f32_16x16x32_bf16 v[44:47], a[194:197], v[96:99], v[44:47]\n"
    "ds_read_b128 v[92:95], v1 offset:64\n"
    "v_sub_f32_dpp v226, v174, v162 row_ror:8 row_mask:0xf bank_mask:0xc\n"
    "v_mfma_f32_16x16x32_bf16 v[48:51], a[202:205], v[96:99], v[48:51]\n"
    "s_add_u32 s54, s47, 0x6cc00\n"
    "s_mov_b32 exec_hi, 0\n"
    "buffer_store_dwordx4 v[240:243], v31, s[24:27], s54 offen offset:1024 sc1 nt\n"
    "s_mov_b32 exec_hi, -1\n"
    "v_mfma_f32_16x16x32_bf16 v[52:55], a[210:213], v[96:99], v[52:55]\n"
    "ds_read_b128 v[96:99], v1 offset:128\n"
    "v_fma_f32 v56, v44, s44, v56\n"
    "v_mfma_f32_16x16x32_bf16 v[32:35], a[6:9], v[76:79], 0\n"
    "s_waitcnt lgkmcnt(5)\n"
    "v_fma_f32 v57, v45, s44, v57\n"
    "v_cvt_pk_bf16_f32 v68, v56, v57\n"
    "v_mfma_f32_16x16x32_bf16 v[36:39], a[14:17], v[76:79], 0\n"
    "v_fma_f32 v58, v46, s44, v58\n"
    "v_fma_f32 v59, v47, s44, v59\n"
    "v_mfma_f32_16x16x32_bf16 v[40:43], a[22:25], v[76:79], 0\n"
    "ds_read_b128 v[76:79], v2 offset:0\n"
    "v_cvt_pk_bf16_f32 v69, v58, v59\n"
    "v_mfma_f32_16x16x32_bf16 v[32:35], a[30:33], v[80:83], v[32:35]\n"
    "v_fma_f32 v60, v48, s44, v60\n"
    "v_fma_f32 v61, v49, s44, v61\n"
    "v_mfma_f32_16x16x32_bf16 v[36:39], a[38:41], v[80:83], v[36:39]\n"
    "s_waitcnt lgkmcnt(4)\n"
    "v_cvt_pk_bf16_f32 v70, v60, v61\n"
    "v_fma_f32 v62, v50, s44, v62\n"
    "v_mfma_f32_16x16x32_bf16 v[40:43], a[46:49], v[80:83], v[40:43]\n"
    "ds_read_b128 v[80:83], v2 offset:64\n"
    "v_fma_f32 v63, v51, s44, v63\n"
    "v_mfma_f32_16x16x32_bf16 v[32:35], a[54:57], v[84:87], v[32:35]\n"
    "v_cvt_pk_bf16_f32 v71, v62, v63\n"
    "v_fma_f32 v64, v52, s44, v64\n"
    "v_mfma_f32_16x16x32_bf16 v[36:39], a[62:65], v[84:87], v[36:39]\n"
    "ds_write_b128 v27, v[68:71] offset:6144\n"
    "v_mfma_f32_16x16x32_bf16 v[40:43], a[70:73], v[84:87], v[40:43]\n"
    "s_waitcnt lgkmcnt(4)\n"
    "ds_read_b128 v[84:87], v2 offset:128\n"
    "v_mfma_f32_16x16x32_bf16 v[32:35], a[78:81], v[88:91], v[32:35]\n"
    "s_waitcnt lgkmcnt(4)\n"
    "v_fma_f32 v65, v53, s44, v65\n"
    "v_cvt_pk_bf16_f32 v72, v64, v65\n"
    "v_mfma_f32_16x16x32_bf16 v[36:39], a[86:89], v[88:91], v[36:39]\n"
    "v_fma_f32 v66, v54, s44, v66\n"
    "v_fma_f32 v67, v55, s44, v67\n"
    "v_mfma_f32_16x16x32_bf16 v[40:43], a[94:97], v[88:91], v[40:43]\n"
    "ds_read_b128 v[88:91], v3 offset:0\n"
    "v_cvt_pk_bf16_f32 v73, v66, v67\n"
    "v_mfma_f32_16x16x32_bf16 v[32:35], a[102:105], v[92:95], v[32:35]\n"
    "ds_write_b64 v28, v[72:73] offset:6144\n"
    "v_mfma_f32_16x16x32_bf16 v[36:39], a[110:113], v[92:95], v[36:39]\n"
    "s_waitcnt lgkmcnt(5)\n"
    "v_add_f32_dpp v227, v163, v175 row_ror:8 row_mask:0xf bank_mask:0x3\n"
    "v_sub_f32_dpp v227, v175, v163 row_ror:8 row_mask:0xf bank_mask:0xc\n"
    "v_mfma_f32_16x16x32_bf16 v[40:43], a[118:121], v[92:95], v[40:43]\n"
    "ds_read_b128 v[92:95], v3 offset:64\n"
    "v_cvt_pk_bf16_f32 v235, v226, v227\n"
    "v_mfma_f32_16x16x32_bf16 v[32:35], a[126:129], v[96:99], v[32:35]\n"
    "s_waitcnt lgkmcnt(5)\n"
    "v_add_f32_dpp v220, v164, v176 row_ror:8 row_mask:0xf bank_mask:0x3\n"
    "v_sub_f32_dpp v220, v176, v164 row_ror:8 row_mask:0xf bank_mask:0xc\n"
    "v_mfma_f32_16x16x32_bf16 v[36:39], a[134:137], v[96:99], v[36:39]\n"
    "v_add_f32_dpp v221, v165, v177 row_ror:8 row_mask:0xf bank_mask:0x3\n"
    "v_sub_f32_dpp v221, v177, v165 row_ror:8 row_mask:0xf bank_mask:0xc\n"
    "v_mfma_f32_16x16x32_bf16 v[40:43], a[142:145], v[96:99], v[40:43]\n"
    "ds_read_b128 v[96:99], v3 offset:128\n"
    "v_cvt_pk_bf16_f32 v236, v220, v221\n"
    "v_mfma_f32_16x16x32_bf16 v[32:35], a[150:153], v[76:79], v[32:35]\n"
    "v_add_f32_dpp v222, v166, v178 row_ror:8 row_mask:0xf bank_mask:0x3\n"
    "v_sub_f32_dpp v222, v178, v166 row_ror:8 row_mask:0xf bank_mask:0xc\n"
    "v_mfma_f32_16x16x32_bf16 v[36:39], a[158:161], v[76:79], v[36:39]\n"
    "s_waitcnt lgkmcnt(4)\n"
    "v_add_f32_dpp v223, v167, v179 row_ror:8 row_mask:0xf bank_mask:0x3\n"
    "v_sub_f32_dpp v223, v179, v167 row_ror:8 row_mask:0xf bank_mask:0xc\n"
    "v_mfma_f32_16x16x32_bf16 v[40:43], a[166:169], v[76:79], v[40:43]\n"
    "ds_read_b128 v[76:79], v4 offset:0\n"
    "v_cvt_pk_bf16_f32 v237, v222, v223\n"
    "v_mfma_f32_16x16x32_bf16 v[32:35], a[174:177], v[80:83], v[32:35]\n"
    "ds_write_b128 v14, v[234:237] offset:0\n"
    "v_mfma_f32_16x16x32_bf16 v[36:39], a[182:185], v[80:83], v[36:39]\n"
    "s_waitcnt lgkmcnt(5)\n"
    "v_add_f32_dpp v224, v168, v180 row_ror:8 row_mask:0xf bank_mask:0x3\n"
    "v_sub_f32_dpp v224, v180, v168 row_ror:8 row_mask:0xf bank_mask:0xc\n"
    "v_mfma_f32_16x16x32_bf16 v[40:43], a[190:193], v[80:83], v[40:43]\n"
    "ds_read_b128 v[80:83], v4 offset:64\n"
    "v_add_f32_dpp v225, v169, v181 row_ror:8 row_mask:0xf bank_mask:0x3\n"
    "v_mfma_f32_16x16x32_bf16 v[32:35], a[198:201], v[84:87], v[32:35]\n"
    "s_waitcnt lgkmcnt(4)\n"
    "v_sub_f32_dpp v225, v181, v169 row_ror:8 row_mask:0xf bank_mask:0xc\n"
    "v_cvt_pk_bf16_f32 v238, v224, v225\n"
    "v_mfma_f32_16x16x32_bf16 v[36:39], a[206:209], v[84:87], v[36:39]\n"
    "v_add_f32_dpp v226, v170, v182 row_ror:8 row_mask:0xf bank_mask:0x3\n"
    "v_sub_f32_dpp v226, v182, v170 row_ror:8 row_mask:0xf bank_mask:0xc\n"
    "v_mfma_f32_16x16x32_bf16 v[40:43], a[214:217], v[84:87], v[40:43]\n"
    "ds_read_b128 v[84:87], v4 offset:128\n"
    "v_add_f32_dpp v227, v171, v183 row_ror:8 row_mask:0xf bank_mask:0x3\n"
    "v_mfma_f32_16x16x32_bf16 v[124:127], a[220:223], v[100:103], v[184:187]\n"
    "ds_write_b128 v16, v[32:35] offset:0\n"
    "v_mfma_f32_16x16x32_bf16 v[128:131], a[224:227], v[100:103], v[188:191]\n"
    "s_waitcnt lgkmcnt(5)\n"
    "v_sub_f32_dpp v227, v183, v171 row_ror:8 row_mask:0xf bank_mask:0xc\n"
    "v_cvt_pk_bf16_f32 v239, v226, v227\n"
    "v_mfma_f32_16x16x32_bf16 v[132:135], a[228:231], v[100:103], v[192:195]\n"
    "ds_read_b128 v[100:103], v7 offset:0\n"
    "v_lshlrev_b32 v184, 16, v202\n"
    "v_mfma_f32_16x16x32_bf16 v[124:127], a[232:235], v[104:107], v[124:127]\n"
    "ds_write_b64 v15, v[238:239] offset:0\n"
    "v_mfma_f32_16x16x32_bf16 v[128:131], a[236:239], v[104:107], v[128:131]\n"
    "s_waitcnt lgkmcnt(6)\n"
    "v_and_b32 v185, 0xffff0000, v202\n"
    "v_lshlrev_b32 v186, 16, v203\n"
    "v_mfma_f32_16x16x32_bf16 v[132:135], a[240:243], v[104:107], v[132:135]\n"
    "ds_read_b128 v[104:107], v7 offset:64\n"
    "v_and_b32 v187, 0xffff0000, v203\n"
    "v_mfma_f32_16x16x32_bf16 v[124:127], a[244:247], v[108:111], v[124:127]\n"
    "ds_write_b128 v16, v[36:39] offset:1024\n"
    "v_mfma_f32_16x16x32_bf16 v[128:131], a[248:251], v[108:111], v[128:131]\n"
    "s_waitcnt lgkmcnt(6)\n"
    "v_lshlrev_b32 v188, 16, v204\n"
    "v_and_b32 v189, 0xffff0000, v204\n"
    "v_mfma_f32_16x16x32_bf16 v[132:135], a[252:255], v[108:111], v[132:135]\n"
    "ds_read_b128 v[108:111], v7 offset:128\n"
    "v_lshlrev_b32 v190, 16, v205\n"
    "v_mfma_f32_16x16x32_bf16 v[44:47], a[2:5], v[88:91], v[32:35]\n"
    "ds_write_b128 v16, v[40:43] offset:2048\n"
    "v_mfma_f32_16x16x32_bf16 v[48:51], a[10:13], v[88:91], v[36:39]\n"
    "s_waitcnt lgkmcnt(7)\n"
    "v_and_b32 v191, 0xffff0000, v205\n"
    "v_lshlrev_b32 v192, 16, v206\n"
    "v_mfma_f32_16x16x32_bf16 v[52:55], a[18:21], v[88:91], v[40:43]\n"
    "ds_read_b128 v[88:91], v5 offset:0\n"
    "v_and_b32 v193, 0xffff0000, v206\n"
    "v_mfma_f32_16x16x32_bf16 v[44:47], a[26:29], v[92:95], v[44:47]\n"
    "s_waitcnt lgkmcnt(6)\n"
    "v_lshlrev_b32 v194, 16, v207\n"
    "v_and_b32 v195, 0xffff0000, v207\n"
    "v_mfma_f32_16x16x32_bf16 v[48:51], a[34:37], v[92:95], v[48:51]\n"
    "v_sub_f32 v220, v136, v124\n"
    "v_sub_f32 v221, v137, v125\n"
    "v_mfma_f32_16x16x32_bf16 v[52:55], a[42:45], v[92:95], v[52:55]\n"
    "ds_read_b128 v[92:95], v5 offset:64\n"
    "s_waitcnt lgkmcnt(2)\n"
    "s_barrier\n"
    "v_mfma_f32_16x16x32_bf16 v[44:47], a[50:53], v[96:99], v[44:47]\n"
    "ds_read_b128 v[56:59], v17 offset:0\n"
    "v_cvt_pk_bf16_f32 v228, v220, v221\n"
    "v_mfma_f32_16x16x32_bf16 v[48:51], a[58:61], v[96:99], v[48:51]\n"
    "ds_read_b128 v[60:63], v17 offset:1024\n"
    "v_sub_f32 v222, v138, v126\n"
    "v_mfma_f32_16x16x32_bf16 v[52:55], a[66:69], v[96:99], v[52:55]\n"
    "ds_read_b128 v[96:99], v5 offset:128\n"
    "v_sub_f32 v223, v139, v127\n"
    "v_mfma_f32_16x16x32_bf16 v[44:47], a[74:77], v[76:79], v[44:47]\n"
    "ds_read_b128 v[64:67], v17 offset:2048\n"
    "v_cvt_pk_bf16_f32 v229, v222, v223\n"
    "v_mfma_f32_16x16x32_bf16 v[48:51], a[82:85], v[76:79], v[48:51]\n"
    "ds_read_b128 v[252:255], v248 offset:6144\n"
    "v_sub_f32 v224, v140, v128\n"
    "v_mfma_f32_16x16x32_bf16 v[52:55], a[90:93], v[76:79], v[52:55]\n"
    "ds_read_b128 v[76:79], v0 offset:3072\n"
    "v_sub_f32 v225, v141, v129\n"
    "v_mfma_f32_16x16x32_bf16 v[44:47], a[98:101], v[80:83], v[44:47]\n"
    "ds_read_b128 v[240:243], v249 offset:6144\n"
    "v_cvt_pk_bf16_f32 v230, v224, v225\n"
    "v_mfma_f32_16x16x32_bf16 v[48:51], a[106:109], v[80:83], v[48:51]\n"
    "s_waitcnt lgkmcnt(8)\n"
    "v_sub_f32 v226, v142, v130\n"
    "v_sub_f32 v227, v143, v131\n"
    "v_mfma_f32_16x16x32_bf16 v[52:55], a[114:117], v[80:83], v[52:55]\n"
    "ds_read_b128 v[80:83], v0 offset:3136\n"
    "v_cvt_pk_bf16_f32 v231, v226, v227\n"
    "v_mfma_f32_16x16x32_bf16 v[44:47], a[122:125], v[84:87], v[44:47]\n"
    "ds_write_b128 v10, v[228:231] offset:0\n"
    "v_mfma_f32_16x16x32_bf16 v[48:51], a[130:133], v[84:87], v[48:51]\n"
    "s_waitcnt lgkmcnt(8)\n"
    "v_sub_f32 v220, v144, v132\n"
    "v_sub_f32 v221, v145, v133\n"
    "v_mfma_f32_16x16x32_bf16 v[52:55], a[138:141], v[84:87], v[52:55]\n"
    "ds_read_b128 v[84:87], v0 offset:3200\n"
    "v_cvt_pk_bf16_f32 v232, v220, v221\n"
    "v_mfma_f32_16x16x32_bf16 v[44:47], a[146:149], v[88:91], v[44:47]\n"
    "s_waitcnt lgkmcnt(5)\n"
    "s_add_u32 s55, s47, 0x6d800\n"
    "buffer_store_dwordx4 v[252:255], v31, s[24:27], s55 offen offset:0 sc1 nt\n"
    "v_mfma_f32_16x16x32_bf16 v[48:51], a[154:157], v[88:91], v[48:51]\n"
    "v_sub_f32 v222, v146, v134\n"
    "v_sub_f32 v223, v147, v135\n"
    "v_mfma_f32_16x16x32_bf16 v[52:55], a[162:165], v[88:91], v[52:55]\n"
    "ds_read_b128 v[88:91], v1 offset:3072\n"
    "v_cvt_pk_bf16_f32 v233, v222, v223\n"
    "v_mfma_f32_16x16x32_bf16 v[44:47], a[170:173], v[92:95], v[44:47]\n"
    "ds_write_b64 v11, v[232:233] offset:0\n"
    "v_mfma_f32_16x16x32_bf16 v[48:51], a[178:181], v[92:95], v[48:51]\n"
    "s_waitcnt lgkmcnt(5)\n"
    "v_add_f32 v224, v148, v124\n"
    "v_add_f32 v225, v149, v125\n"
    "v_mfma_f32_16x16x32_bf16 v[52:55], a[186:189], v[92:95], v[52:55]\n"
    "ds_read_b128 v[92:95], v1 offset:3136\n"
    "v_cvt_pk_bf16_f32 v234, v224, v225\n"
    "v_mfma_f32_16x16x32_bf16 v[44:47], a[194:197], v[96:99], v[44:47]\n"
    "s_add_u32 s52, s47, 0x6d800\n"
    "s_mov_b32 exec_hi, 0\n"
    "buffer_store_dwordx4 v[240:243], v31, s[24:27], s52 offen offset:1024 sc1 nt\n"
    "s_mov_b32 exec_hi, -1\n"
    "v_mfma_f32_16x16x32_bf16 v[48:51], a[202:205], v[96:99], v[48:51]\n"
    "ds_read_b128 v[240:243], v29 offset:6144\n"
    "v_add_f32 v226, v150, v126\n"
    "v_mfma_f32_16x16x32_bf16 v[52:55], a[210:213], v[96:99], v[52:55]\n"
    "ds_read_b128 v[96:99], v1 offset:3200\n"
    "v_fma_f32 v56, v44, s44, v56\n"
    "v_mfma_f32_16x16x32_bf16 v[32:35], a[6:9], v[76:79], 0\n"
    "s_waitcnt lgkmcnt(5)\n"
    "v_fma_f32 v57, v45, s44, v57\n"
    "v_cvt_pk_bf16_f32 v68, v56, v57\n"
    "v_mfma_f32_16x16x32_bf16 v[36:39], a[14:17], v[76:79], 0\n"
    "v_fma_f32 v58, v46, s44, v58\n"
    "v_fma_f32 v59, v47, s44, v59\n"
    "v_mfma_f32_16x16x32_bf16 v[40:43], a[22:25], v[76:79], 0\n"
    "ds_read_b128 v[76:79], v2 offset:3072\n"
    "v_cvt_pk_bf16_f32 v69, v58, v59\n"
    "v_mfma_f32_16x16x32_bf16 v[32:35], a[30:33], v[80:83], v[32:35]\n"
    "s_waitcnt lgkmcnt(5)\n"
    "v_fma_f32 v60, v48, s44, v60\n"
    "v_fma_f32 v61, v49, s44, v61\n"
    "v_mfma_f32_16x16x32_bf16 v[36:39], a[38:41], v[80:83], v[36:39]\n"
    "v_cvt_pk_bf16_f32 v70, v60, v61\n"
    "v_fma_f32 v62, v50, s44, v62\n"
    "v_mfma_f32_16x16x32_bf16 v[40:43], a[46:49], v[80:83], v[40:43]\n"
    "ds_read_b128 v[80:83], v2 offset:3136\n"
    "v_fma_f32 v63, v51, s44, v63\n"
    "v_mfma_f32_16x16x32_bf16 v[32:35], a[54:57], v[84:87], v[32:35]\n"
    "s_waitcnt lgkmcnt(4)\n"
    "v_cvt_pk_bf16_f32 v71, v62, v63\n"
    "v_fma_f32 v64, v52, s44, v64\n"
    "v_mfma_f32_16x16x32_bf16 v[36:39], a[62:65], v[84:87], v[36:39]\n"
    "ds_write_b128 v27, v[68:71] offset:0\n"
    "v_mfma_f32_16x16x32_bf16 v[40:43], a[70:73], v[84:87], v[40:43]\n"
    "s_waitcnt lgkmcnt(4)\n"
    "ds_read_b128 v[84:87], v2 offset:3200\n"
    "v_mfma_f32_16x16x32_bf16 v[32:35], a[78:81], v[88:91], v[32:35]\n"
    "s_waitcnt lgkmcnt(4)\n"
    "v_fma_f32 v65, v53, s44, v65\n"
    "v_cvt_pk_bf16_f32 v72, v64, v65\n"
    "v_mfma_f32_16x16x32_bf16 v[36:39], a[86:89], v[88:91], v[36:39]\n"
    "ds_write_b128 v30, v[240:243] offset:38400\n"
    "v_mfma_f32_16x16x32_bf16 v[40:43], a[94:97], v[88:91], v[40:43]\n"
    "ds_read_b128 v[88:91], v3 offset:3072\n"
    "v_mfma_f32_16x16x32_bf16 v[32:35], a[102:105], v[92:95], v[32:35]\n"
    "s_waitcnt lgkmcnt(5)\n"
    "v_fma_f32 v66, v54, s44, v66\n"
    "v_fma_f32 v67, v55, s44, v67\n"
    "v_mfma_f32_16x16x32_bf16 v[36:39], a[110:113], v[92:95], v[36:39]\n"
    "v_cvt_pk_bf16_f32 v73, v66, v67\n"
    "v_add_f32 v227, v151, v127\n"
    "v_mfma_f32_16x16x32_bf16 v[40:43], a[118:121], v[92:95], v[40:43]\n"
    "ds_read_b128 v[92:95], v3 offset:3136\n"
    "v_cvt_pk_bf16_f32 v235, v226, v227\n"
    "v_mfma_f32_16x16x32_bf16 v[32:35], a[126:129], v[96:99], v[32:35]\n"
    "ds_write_b64 v28, v[72:73] offset:0\n"
    "v_mfma_f32_16x16x32_bf16 v[36:39], a[134:137], v[96:99], v[36:39]\n"
    "s_waitcnt lgkmcnt(6)\n"
    "v_add_f32 v220, v152, v128\n"
    "v_add_f32 v221, v153, v129\n"
    "v_mfma_f32_16x16x32_bf16 v[40:43], a[142:145], v[96:99], v[40:43]\n"
    "ds_read_b128 v[96:99], v3 offset:3200\n"
    "v_cvt_pk_bf16_f32 v236, v220, v221\n"
    "v_mfma_f32_16x16x32_bf16 v[32:35], a[150:153], v[76:79], v[32:35]\n"
    "s_waitcnt lgkmcnt(5)\n"
    "v_add_f32 v222, v154, v130\n"
    "v_add_f32 v223, v155, v131\n"
    "v_mfma_f32_16x16x32_bf16 v[36:39], a[158:161], v[76:79], v[36:39]\n"
    "v_cvt_pk_bf16_f32 v237, v222, v223\n"
    "v_add_f32 v224, v156, v132\n"
    "v_mfma_f32_16x16x32_bf16 v[40:43], a[166:169], v[76:79], v[40:43]\n"
    "ds_read_b128 v[76:79], v4 offset:3072\n"
    "v_add_f32 v225, v157, v133\n"
    "v_mfma_f32_16x16x32_bf16 v[32:35], a[174:177], v[80:83], v[32:35]\n"
    "ds_write_b128 v10, v[234:237] offset:9600\n"
    "v_mfma_f32_16x16x32_bf16 v[36:39], a[182:185], v[80:83], v[36:39]\n"
    "s_waitcnt lgkmcnt(5)\n"
    "v_cvt_pk_bf16_f32 v238, v224, v225\n"
    "v_add_f32 v226, v158, v134\n"
    "v_mfma_f32_16x16x32_bf16 v[40:43], a[190:193], v[80:83], v[40:43]\n"
    "ds_read_b128 v[80:83], v4 offset:3136\n"
    "v_add_f32 v227, v159, v135\n"
    "v_mfma_f32_16x16x32_bf16 v[32:35], a[198:201], v[84:87], v[32:35]\n"
    "s_waitcnt lgkmcnt(5)\n"
    "v_cvt_pk_bf16_f32 v239, v226, v227\n"
    "v_sub_f32 v220, v124, v148\n"
    "v_mfma_f32_16x16x32_bf16 v[36:39], a[206:209], v[84:87], v[36:39]\n"
    "ds_write_b64 v11, v[238:239] offset:9600\n"
    "v_mfma_f32_16x16x32_bf16 v[40:43], a[214:217], v[84:87], v[40:43]\n"
    "v_sub_f32 v221, v125, v149\n"
    "v_cvt_pk_bf16_f32 v228, v220, v221\n"
    "v_mfma_f32_16x16x32_bf16 v[136:139], a[220:223], v[112:115], v[184:187]\n"
    "ds_read_b128 v[84:87], v4 offset:3200\n"
    "v_sub_f32 v222, v126, v150\n"
    "v_mfma_f32_16x16x32_bf16 v[140:143], a[224:227], v[112:115], v[188:191]\n"
    "ds_write_b128 v16, v[32:35] offset:12288\n"
    "v_mfma_f32_16x16x32_bf16 v[144:147], a[228:231], v[112:115], v[192:195]\n"
    "s_waitcnt lgkmcnt(5)\n"
    "v_sub_f32 v223, v127, v151\n"
    "v_cvt_pk_bf16_f32 v229, v222, v223\n"
    "v_mfma_f32_16x16x32_bf16 v[136:139], a[232:235], v[116:119], v[136:139]\n"
    "v_sub_f32 v224, v128, v152\n"
    "v_sub_f32 v225, v129, v153\n"
    "v_mfma_f32_16x16x32_bf16 v[140:143], a[236:239], v[116:119], v[140:143]\n"
    "ds_write_b128 v16, v[36:39] offset:13312\n"
    "v_mfma_f32_16x16x32_bf16 v[144:147], a[240:243], v[116:119], v[144:147]\n"
    "s_waitcnt lgkmcnt(4)\n"
    "v_cvt_pk_bf16_f32 v230, v224, v225\n"
    "v_sub_f32 v226, v130, v154\n"
    "v_mfma_f32_16x16x32_bf16 v[136:139], a[244:247], v[120:123], v[136:139]\n"
    "v_sub_f32 v227, v131, v155\n"
    "v_cvt_pk_bf16_f32 v231, v226, v227\n"
    "v_mfma_f32_16x16x32_bf16 v[140:143], a[248:251], v[120:123], v[140:143]\n"
    "ds_write_b128 v16, v[40:43] offset:14336\n"
    "v_mfma_f32_16x16x32_bf16 v[144:147], a[252:255], v[120:123], v[144:147]\n"
    "v_sub_f32 v220, v132, v156\n"
    "v_sub_f32 v221, v133, v157\n"
    "v_mfma_f32_16x16x32_bf16 v[44:47], a[2:5], v[88:91], v[32:35]\n"
    "s_waitcnt lgkmcnt(3)\n"
    "v_cvt_pk_bf16_f32 v232, v220, v221\n"
    "v_sub_f32 v222, v134, v158\n"
    "v_mfma_f32_16x16x32_bf16 v[48:51], a[10:13], v[88:91], v[36:39]\n"
    "ds_write_b128 v10, v[228:231] offset:19200\n"
    "v_mfma_f32_16x16x32_bf16 v[52:55], a[18:21], v[88:91], v[40:43]\n"
    "ds_read_b128 v[88:91], v5 offset:3072\n"
    "v_mfma_f32_16x16x32_bf16 v[44:47], a[26:29], v[92:95], v[44:47]\n"
    "s_waitcnt lgkmcnt(2)\n"
    "s_barrier\n"
    "v_sub_f32 v223, v135, v159\n"
    "v_mfma_f32_16x16x32_bf16 v[48:51], a[34:37], v[92:95], v[48:51]\n"
    "ds_read_b128 v[56:59], v17 offset:12288\n"
    "v_cvt_pk_bf16_f32 v233, v222, v223\n"
    "v_mfma_f32_16x16x32_bf16 v[52:55], a[42:45], v[92:95], v[52:55]\n"
    "ds_read_b128 v[92:95], v5 offset:3136\n"
    "v_sub_f32 v224, v148, v136\n"
    "v_mfma_f32_16x16x32_bf16 v[44:47], a[50:53], v[96:99], v[44:47]\n"
    "ds_read_b128 v[60:63], v17 offset:13312\n"
    "v_sub_f32 v225, v149, v137\n"
    "v_mfma_f32_16x16x32_bf16 v[48:51], a[58:61], v[96:99], v[48:51]\n"
    "ds_read_b128 v[64:67], v17 offset:14336\n"
    "v_cvt_pk_bf16_f32 v234, v224, v225\n"
    "v_mfma_f32_16x16x32_bf16 v[52:55], a[66:69], v[96:99], v[52:55]\n"
    "ds_read_b128 v[96:99], v5 offset:3200\n"
    "v_sub_f32 v226, v150, v138\n"
    "v_mfma_f32_16x16x32_bf16 v[44:47], a[74:77], v[76:79], v[44:47]\n"
    "ds_read_b128 v[252:255], v248 offset:0\n"
    "v_sub_f32 v227, v151, v139\n"
    "v_mfma_f32_16x16x32_bf16 v[48:51], a[82:85], v[76:79], v[48:51]\n"
    "ds_read_b128 v[240:243], v249 offset:0\n"
    "v_cvt_pk_bf16_f32 v235, v226, v227\n"
    "v_mfma_f32_16x16x32_bf16 v[52:55], a[90:93], v[76:79], v[52:55]\n"
    "ds_read_b128 v[76:79], v0 offset:6144\n"
    "v_sub_f32 v220, v152, v140\n"
    "v_mfma_f32_16x16x32_bf16 v[44:47], a[98:101], v[80:83], v[44:47]\n"
    "ds_write_b64 v11, v[232:233] offset:19200\n"
    "v_mfma_f32_16x16x32_bf16 v[48:51], a[106:109], v[80:83], v[48:51]\n"
    "s_waitcnt lgkmcnt(9)\n"
    "v_sub_f32 v221, v153, v141\n"
    "v_cvt_pk_bf16_f32 v236, v220, v221\n"
    "v_mfma_f32_16x16x32_bf16 v[52:55], a[114:117], v[80:83], v[52:55]\n"
    "ds_read_b128 v[80:83], v0 offset:6208\n"
    "v_sub_f32 v222, v154, v142\n"
    "v_mfma_f32_16x16x32_bf16 v[44:47], a[122:125], v[84:87], v[44:47]\n"
    "s_waitcnt lgkmcnt(9)\n"
    "v_sub_f32 v223, v155, v143\n"
    "v_cvt_pk_bf16_f32 v237, v222, v223\n"
    "v_mfma_f32_16x16x32_bf16 v[48:51], a[130:133], v[84:87], v[48:51]\n"
    "ds_write_b128 v10, v[234:237] offset:28800\n"
    "v_mfma_f32_16x16x32_bf16 v[52:55], a[138:141], v[84:87], v[52:55]\n"
    "s_waitcnt lgkmcnt(5)\n"
    "s_add_u32 s53, s47, 0x90000\n"
    "buffer_store_dwordx4 v[252:255], v31, s[24:27], s53 offen offset:0 sc1 nt\n"
    "v_mfma_f32_16x16x32_bf16 v[44:47], a[146:149], v[88:91], v[44:47]\n"
    "ds_read_b128 v[84:87], v0 offset:6272\n"
    "v_sub_f32 v224, v156, v144\n"
    "v_mfma_f32_16x16x32_bf16 v[48:51], a[154:157], v[88:91], v[48:51]\n"
    "s_waitcnt lgkmcnt(5)\n"
    "v_sub_f32 v225, v157, v145\n"
    "v_cvt_pk_bf16_f32 v238, v224, v225\n"
    "v_mfma_f32_16x16x32_bf16 v[52:55], a[162:165], v[88:91], v[52:55]\n"
    "ds_read_b128 v[88:91], v1 offset:6144\n"
    "v_sub_f32 v226, v158, v146\n"
    "v_mfma_f32_16x16x32_bf16 v[44:47], a[170:173], v[92:95], v[44:47]\n"
    "s_waitcnt lgkmcnt(5)\n"
    "v_sub_f32 v227, v159, v147\n"
    "v_cvt_pk_bf16_f32 v239, v226, v227\n"
    "v_mfma_f32_16x16x32_bf16 v[48:51], a[178:181], v[92:95], v[48:51]\n"
    "s_add_u32 s54, s47, 0x90000\n"
    "s_mov_b32 exec_hi, 0\n"
    "buffer_store_dwordx4 v[240:243], v31, s[24:27], s54 offen offset:1024 sc1 nt\n"
    "s_mov_b32 exec_hi, -1\n"
    "v_mfma_f32_16x16x32_bf16 v[52:55], a[186:189], v[92:95], v[52:55]\n"
    "ds_read_b128 v[92:95], v1 offset:6208\n"
    "v_lshlrev_b32 v160, 16, v208\n"
    "v_mfma_f32_16x16x32_bf16 v[44:47], a[194:197], v[96:99], v[44:47]\n"
    "ds_write_b64 v11, v[238:239] offset:28800\n"
    "v_mfma_f32_16x16x32_bf16 v[48:51], a[202:205], v[96:99], v[48:51]\n"
    "s_waitcnt lgkmcnt(5)\n"
    "v_and_b32 v161, 0xffff0000, v208\n"
    "v_lshlrev_b32 v162, 16, v209\n"
    "v_mfma_f32_16x16x32_bf16 v[52:55], a[210:213], v[96:99], v[52:55]\n"
    "ds_read_b128 v[96:99], v1 offset:6272\n"
    "s_nop 0\n"
    "v_fma_f32 v56, v44, s44, v56\n"
    "v_mfma_f32_16x16x32_bf16 v[32:35], a[6:9], v[76:79], 0\n"
    "v_fma_f32 v57, v45, s44, v57\n"
    "v_cvt_pk_bf16_f32 v68, v56, v57\n"
    "v_mfma_f32_16x16x32_bf16 v[36:39], a[14:17], v[76:79], 0\n"
    "s_waitcnt lgkmcnt(4)\n"
    "v_fma_f32 v58, v46, s44, v58\n"
    "v_fma_f32 v59, v47, s44, v59\n"
    "v_mfma_f32_16x16x32_bf16 v[40:43], a[22:25], v[76:79], 0\n"
    "ds_read_b128 v[76:79], v2 offset:6144\n"
    "v_cvt_pk_bf16_f32 v69, v58, v59\n"
    "v_mfma_f32_16x16x32_bf16 v[32:35], a[30:33], v[80:83], v[32:35]\n"
    "s_waitcnt lgkmcnt(4)\n"
    "v_fma_f32 v60, v48, s44, v60\n"
    "v_fma_f32 v61, v49, s44, v61\n"
    "v_mfma_f32_16x16x32_bf16 v[36:39], a[38:41], v[80:83], v[36:39]\n"
    "v_cvt_pk_bf16_f32 v70, v60, v61\n"
    "v_fma_f32 v62, v50, s44, v62\n"
    "v_mfma_f32_16x16x32_bf16 v[40:43], a[46:49], v[80:83], v[40:43]\n"
    "ds_read_b128 v[80:83], v2 offset:6208\n"
    "v_fma_f32 v63, v51, s44, v63\n"
    "v_mfma_f32_16x16x32_bf16 v[32:35], a[54:57], v[84:87], v[32:35]\n"
    "v_cvt_pk_bf16_f32 v71, v62, v63\n"
    "v_fma_f32 v64, v52, s44, v64\n"
    "v_mfma_f32_16x16x32_bf16 v[36:39], a[62:65], v[84:87], v[36:39]\n"
    "ds_write_b128 v27, v[68:71] offset:6144\n"
    "v_mfma_f32_16x16x32_bf16 v[40:43], a[70:73], v[84:87], v[40:43]\n"
    "s_waitcnt lgkmcnt(5)\n"
    "ds_read_b128 v[84:87], v2 offset:6272\n"
    "v_mfma_f32_16x16x32_bf16 v[32:35], a[78:81], v[88:91], v[32:35]\n"
    "s_waitcnt lgkmcnt(4)\n"
    "v_fma_f32 v65, v53, s44, v65\n"
    "v_cvt_pk_bf16_f32 v72, v64, v65\n"
    "v_mfma_f32_16x16x32_bf16 v[36:39], a[86:89], v[88:91], v[36:39]\n"
    "v_fma_f32 v66, v54, s44, v66\n"
    "v_fma_f32 v67, v55, s44, v67\n"
    "v_mfma_f32_16x16x32_bf16 v[40:43], a[94:97], v[88:91], v[40:43]\n"
    "ds_read_b128 v[88:91], v3 offset:6144\n"
    "v_cvt_pk_bf16_f32 v73, v66, v67\n"
    "v_mfma_f32_16x16x32_bf16 v[32:35], a[102:105], v[92:95], v[32:35]\n"
    "ds_write_b64 v28, v[72:73] offset:6144\n"
    "v_mfma_f32_16x16x32_bf16 v[36:39], a[110:113], v[92:95], v[36:39]\n"
    "s_waitcnt lgkmcnt(5)\n"
    "v_and_b32 v163, 0xffff0000, v209\n"
    "v_lshlrev_b32 v164, 16, v210\n"
    "v_mfma_f32_16x16x32_bf16 v[40:43], a[118:121], v[92:95], v[40:43]\n"
    "ds_read_b128 v[92:95], v3 offset:6208\n"
    "v_and_b32 v165, 0xffff0000, v210\n"
    "v_mfma_f32_16x16x32_bf16 v[32:35], a[126:129], v[96:99], v[32:35]\n"
    "s_waitcnt lgkmcnt(5)\n"
    "v_lshlrev_b32 v166, 16, v211\n"
    "v_and_b32 v167, 0xffff0000, v211\n"
    "v_mfma_f32_16x16x32_bf16 v[36:39], a[134:137], v[96:99], v[36:39]\n"
    "v_lshlrev_b32 v168, 16, v212\n"
    "v_and_b32 v169, 0xffff0000, v212\n"
    "v_mfma_f32_16x16x32_bf16 v[40:43], a[142:145], v[96:99], v[40:43]\n"
    "ds_read_b128 v[96:99], v3 offset:6272\n"
    "v_lshlrev_b32 v170, 16, v213\n"
    "v_mfma_f32_16x16x32_bf16 v[32:35], a[150:153], v[76:79], v[32:35]\n"
    "v_and_b32 v171, 0xffff0000, v213\n"
    "v_mfma_f32_16x16x32_bf16 v[36:39], a[158:161], v[76:79], v[36:39]\n"
    "s_waitcnt lgkmcnt(4)\n"
    "v_mfma_f32_16x16x32_bf16 v[40:43], a[166:169], v[76:79], v[40:43]\n"
    "ds_read_b128 v[76:79], v4 offset:6144\n"
    "v_mfma_f32_16x16x32_bf16 v[32:35], a[174:177], v[80:83], v[32:35]\n"
    "v_mfma_f32_16x16x32_bf16 v[36:39], a[182:185], v[80:83], v[36:39]\n"
    "s_waitcnt lgkmcnt(4)\n"
    "v_mfma_f32_16x16x32_bf16 v[40:43], a[190:193], v[80:83], v[40:43]\n"
    "ds_read_b128 v[80:83], v4 offset:6208\n"
    "v_mfma_f32_16x16x32_bf16 v[32:35], a[198:201], v[84:87], v[32:35]\n"
    "v_mfma_f32_16x16x32_bf16 v[36:39], a[206:209], v[84:87], v[36:39]\n"
    "s_waitcnt lgkmcnt(3)\n"
    "v_mfma_f32_16x16x32_bf16 v[40:43], a[214:217], v[84:87], v[40:43]\n"
    "ds_read_b128 v[84:87], v4 offset:6272\n"
    "v_mfma_f32_16x16x32_bf16 v[160:163], a[220:223], v[100:103], v[160:163]\n"
    "s_nop 2\n"
    "ds_write_b128 v16, v[32:35] offset:0\n"
    "v_mfma_f32_16x16x32_bf16 v[164:167], a[224:227], v[100:103], v[164:167]\n"
    "s_waitcnt lgkmcnt(4)\n"
    "v_mfma_f32_16x16x32_bf16 v[168:171], a[228:231], v[100:103], v[168:171]\n"
    "v_mfma_f32_16x16x32_bf16 v[160:163], a[232:235], v[104:107], v[160:163]\n"
    "ds_write_b128 v16, v[36:39] offset:1024\n"
    "v_mfma_f32_16x16x32_bf16 v[164:167], a[236:239], v[104:107], v[164:167]\n"
    "s_waitcnt lgkmcnt(4)\n"
    "v_mfma_f32_16x16x32_bf16 v[168:171], a[240:243], v[104:107], v[168:171]\n"
    "s_waitcnt lgkmcnt(3)\n"
    "v_mfma_f32_16x16x32_bf16 v[160:163], a[244:247], v[108:111], v[160:163]\n"
    "ds_write_b128 v16, v[40:43] offset:2048\n"
    "v_mfma_f32_16x16x32_bf16 v[164:167], a[248:251], v[108:111], v[164:167]\n"
    "v_mfma_f32_16x16x32_bf16 v[168:171], a[252:255], v[108:111], v[168:171]\n"
    "s_waitcnt lgkmcnt(3)\n"
    "s_nop 3\n"
    "v_sub_f32 v220, v172, v160\n"
    "v_sub_f32 v221, v173, v161\n"
    "v_mfma_f32_16x16x32_bf16 v[44:47], a[2:5], v[88:91], v[32:35]\n"
    "v_cvt_pk_bf16_f32 v228, v220, v221\n"
    "v_sub_f32 v222, v174, v162\n"
    "v_mfma_f32_16x16x32_bf16 v[48:51], a[10:13], v[88:91], v[36:39]\n"
    "v_sub_f32 v223, v175, v163\n"
    "v_cvt_pk_bf16_f32 v229, v222, v223\n"
    "v_mfma_f32_16x16x32_bf16 v[52:55], a[18:21], v[88:91], v[40:43]\n"
    "ds_read_b128 v[88:91], v5 offset:6144\n"
    "s_waitcnt lgkmcnt(1)\n"
    "s_barrier\n"
    "v_mfma_f32_16x16x32_bf16 v[44:47], a[26:29], v[92:95], v[44:47]\n"
    "ds_read_b128 v[56:59], v17 offset:0\n"
    "v_sub_f32 v224, v176, v164\n"
    "v_mfma_f32_16x16x32_bf16 v[48:51], a[34:37], v[92:95], v[48:51]\n"
    "ds_read_b128 v[60:63], v17 offset:1024\n"
    "v_sub_f32 v225, v177, v165\n"
    "v_mfma_f32_16x16x32_bf16 v[52:55], a[42:45], v[92:95], v[52:55]\n"
    "ds_read_b128 v[92:95], v5 offset:6208\n"
    "v_cvt_pk_bf16_f32 v230, v224, v225\n"
    "v_mfma_f32_16x16x32_bf16 v[44:47], a[50:53], v[96:99], v[44:47]\n"
    "ds_read_b128 v[64:67], v17 offset:2048\n"
    "v_sub_f32 v226, v178, v166\n"
    "v_mfma_f32_16x16x32_bf16 v[48:51], a[58:61], v[96:99], v[48:51]\n"
    "ds_read_b128 v[252:255], v248 offset:6144\n"
    "v_sub_f32 v227, v179, v167\n"
    "v_mfma_f32_16x16x32_bf16 v[52:55], a[66:69], v[96:99], v[52:55]\n"
    "ds_read_b128 v[96:99], v5 offset:6272\n"
    "v_cvt_pk_bf16_f32 v231, v226, v227\n"
    "v_mfma_f32_16x16x32_bf16 v[44:47], a[74:77], v[76:79], v[44:47]\n"
    "ds_read_b128 v[240:243], v249 offset:6144\n"
    "v_sub_f32 v220, v180, v168\n"
    "v_mfma_f32_16x16x32_bf16 v[48:51], a[82:85], v[76:79], v[48:51]\n"
    "ds_write_b128 v12, v[228:231] offset:38400\n"
    "v_mfma_f32_16x16x32_bf16 v[52:55], a[90:93], v[76:79], v[52:55]\n"
    "ds_read_b128 v[76:79], v0 offset:38400\n"
    "v_mfma_f32_16x16x32_bf16 v[44:47], a[98:101], v[80:83], v[44:47]\n"
    "s_waitcnt lgkmcnt(9)\n"
    "v_sub_f32 v221, v181, v169\n"
    "v_cvt_pk_bf16_f32 v232, v220, v221\n"
    "v_mfma_f32_16x16x32_bf16 v[48:51], a[106:109], v[80:83], v[48:51]\n"
    "s_waitcnt lgkmcnt(8)\n"
    "v_sub_f32 v222, v182, v170\n"
    "v_sub_f32 v223, v183, v171\n"
    "v_mfma_f32_16x16x32_bf16 v[52:55], a[114:117], v[80:83], v[52:55]\n"
    "ds_read_b128 v[80:83], v0 offset:38464\n"
    "v_cvt_pk_bf16_f32 v233, v222, v223\n"
    "v_mfma_f32_16x16x32_bf16 v[44:47], a[122:125], v[84:87], v[44:47]\n"
    "s_waitcnt lgkmcnt(5)\n"
    "s_add_u32 s55, s47, 0x90c00\n"
    "buffer_store_dwordx4 v[252:255], v31, s[24:27], s55 offen offset:0 sc1 nt\n"
    "v_mfma_f32_16x16x32_bf16 v[48:51], a[130:133], v[84:87], v[48:51]\n"
    "ds_write_b64 v13, v[232:233] offset:38400\n"
    "v_mfma_f32_16x16x32_bf16 v[52:55], a[138:141], v[84:87], v[52:55]\n"
    "s_waitcnt lgkmcnt(5)\n"
    "v_add_f32_dpp v224, v172, v160 row_ror:8 row_mask:0xf bank_mask:0x3\n"
    "v_sub_f32_dpp v224, v160, v172 row_ror:8 row_mask:0xf bank_mask:0xc\n"
    "v_mfma_f32_16x16x32_bf16 v[44:47], a[146:149], v[88:91], v[44:47]\n"
    "ds_read_b128 v[84:87], v0 offset:38528\n"
    "v_add_f32_dpp v225, v173, v161 row_ror:8 row_mask:0xf bank_mask:0x3\n"
    "v_mfma_f32_16x16x32_bf16 v[48:51], a[154:157], v[88:91], v[48:51]\n"
    "s_waitcnt lgkmcnt(5)\n"
    "v_sub_f32_dpp v225, v161, v173 row_ror:8 row_mask:0xf bank_mask:0xc\n"
    "v_cvt_pk_bf16_f32 v234, v224, v225\n"
    "v_mfma_f32_16x16x32_bf16 v[52:55], a[162:165], v[88:91], v[52:55]\n"
    "ds_read_b128 v[88:91], v1 offset:38400\n"
    "v_add_f32_dpp v226, v174, v162 row_ror:8 row_mask:0xf bank_mask:0x3\n"
    "v_mfma_f32_16x16x32_bf16 v[44:47], a[170:173], v[92:95], v[44:47]\n"
    "s_add_u32 s52, s47, 0x90c00\n"
    "s_mov_b32 exec_hi, 0\n"
    "buffer_store_dwordx4 v[240:243], v31, s[24:27], s52 offen offset:1024 sc1 nt\n"
    "s_mov_b32 exec_hi, -1\n"
    "v_mfma_f32_16x16x32_bf16 v[48:51], a[178:181], v[92:95], v[48:51]\n"
    "s_waitcnt lgkmcnt(4)\n"
    "v_sub_f32_dpp v226, v162, v174 row_ror:8 row_mask:0xf bank_mask:0xc\n"
    "v_add_f32_dpp v227, v175, v163 row_ror:8 row_mask:0xf bank_mask:0x3\n"
    "v_mfma_f32_16x16x32_bf16 v[52:55], a[186:189], v[92:95], v[52:55]\n"
    "ds_read_b128 v[92:95], v1 offset:38464\n"
    "v_sub_f32_dpp v227, v163, v175 row_ror:8 row_mask:0xf bank_mask:0xc\n"
    "v_mfma_f32_16x16x32_bf16 v[44:47], a[194:197], v[96:99], v[44:47]\n"
    "s_waitcnt lgkmcnt(4)\n"
    "v_cvt_pk_bf16_f32 v235, v226, v227\n"
    "v_add_f32_dpp v220, v176, v164 row_ror:8 row_mask:0xf bank_mask:0x3\n"
    "v_mfma_f32_16x16x32_bf16 v[48:51], a[202:205], v[96:99], v[48:51]\n"
    "v_sub_f32_dpp v220, v164, v176 row_ror:8 row_mask:0xf bank_mask:0xc\n"
    "v_add_f32_dpp v221, v177, v165 row_ror:8 row_mask:0xf bank_mask:0x3\n"
    "v_mfma_f32_16x16x32_bf16 v[52:55], a[210:213], v[96:99], v[52:55]\n"
    "ds_read_b128 v[96:99], v1 offset:38528\n"
    "v_sub_f32_dpp v221, v165, v177 row_ror:8 row_mask:0xf bank_mask:0xc\n"
    "v_mfma_f32_16x16x32_bf16 v[32:35], a[6:9], v[76:79], 0\n"
    "v_fma_f32 v56, v44, s44, v56\n"
    "v_fma_f32 v57, v45, s44, v57\n"
    "v_mfma_f32_16x16x32_bf16 v[36:39], a[14:17], v[76:79], 0\n"
    "s_waitcnt lgkmcnt(3)\n"
    "v_cvt_pk_bf16_f32 v68, v56, v57\n"
    "v_fma_f32 v58, v46, s44, v58\n"
    "v_mfma_f32_16x16x32_bf16 v[40:43], a[22:25], v[76:79], 0\n"
    "ds_read_b128 v[76:79], v2 offset:38400\n"
    "v_fma_f32 v59, v47, s44, v59\n"
    "v_mfma_f32_16x16x32_bf16 v[32:35], a[30:33], v[80:83], v[32:35]\n"
    "s_waitcnt lgkmcnt(3)\n"
    "v_cvt_pk_bf16_f32 v69, v58, v59\n"
    "v_fma_f32 v60, v48, s44, v60\n"
    "v_mfma_f32_16x16x32_bf16 v[36:39], a[38:41], v[80:83], v[36:39]\n"
    "v_fma_f32 v61, v49, s44, v61\n"
    "v_cvt_pk_bf16_f32 v70, v60, v61\n"
    "v_mfma_f32_16x16x32_bf16 v[40:43], a[46:49], v[80:83], v[40:43]\n"
    "ds_read_b128 v[80:83], v2 offset:38464\n"
    "v_fma_f32 v62, v50, s44, v62\n"
    "v_mfma_f32_16x16x32_bf16 v[32:35], a[54:57], v[84:87], v[32:35]\n"
    "v_fma_f32 v63, v51, s44, v63\n"
    "v_cvt_pk_bf16_f32 v71, v62, v63\n"
    "v_mfma_f32_16x16x32_bf16 v[36:39], a[62:65], v[84:87], v[36:39]\n"
    "ds_write_b128 v27, v[68:71] offset:0\n"
    "v_mfma_f32_16x16x32_bf16 v[40:43], a[70:73], v[84:87], v[40:43]\n"
    "s_waitcnt lgkmcnt(4)\n"
    "ds_read_b128 v[84:87], v2 offset:38528\n"
    "v_mfma_f32_16x16x32_bf16 v[32:35], a[78:81], v[88:91], v[32:35]\n"
    "s_waitcnt lgkmcnt(4)\n"
    "v_fma_f32 v64, v52, s44, v64\n"
    "v_fma_f32 v65, v53, s44, v65\n"
    "v_mfma_f32_16x16x32_bf16 v[36:39], a[86:89], v[88:91], v[36:39]\n"
    "v_cvt_pk_bf16_f32 v72, v64, v65\n"
    "v_fma_f32 v66, v54, s44, v66\n"
    "v_mfma_f32_16x16x32_bf16 v[40:43], a[94:97], v[88:91], v[40:43]\n"
    "ds_read_b128 v[88:91], v3 offset:38400\n"
    "v_fma_f32 v67, v55, s44, v67\n"
    "v_mfma_f32_16x16x32_bf16 v[32:35], a[102:105], v[92:95], v[32:35]\n"
    "s_waitcnt lgkmcnt(4)\n"
    "v_cvt_pk_bf16_f32 v73, v66, v67\n"
    "v_cvt_pk_bf16_f32 v236, v220, v221\n"
    "v_mfma_f32_16x16x32_bf16 v[36:39], a[110:113], v[92:95], v[36:39]\n"
    "ds_write_b64 v28, v[72:73] offset:0\n"
    "v_mfma_f32_16x16x32_bf16 v[40:43], a[118:121], v[92:95], v[40:43]\n"
    "v_add_f32_dpp v222, v178, v166 row_ror:8 row_mask:0xf bank_mask:0x3\n"
    "v_sub_f32_dpp v222, v166, v178 row_ror:8 row_mask:0xf bank_mask:0xc\n"
    "v_mfma_f32_16x16x32_bf16 v[32:35], a[126:129], v[96:99], v[32:35]\n"
    "ds_read_b128 v[92:95], v3 offset:38464\n"
    "v_add_f32_dpp v223, v179, v167 row_ror:8 row_mask:0xf bank_mask:0x3\n"
    "v_mfma_f32_16x16x32_bf16 v[36:39], a[134:137], v[96:99], v[36:39]\n"
    "s_waitcnt lgkmcnt(5)\n"
    "v_sub_f32_dpp v223, v167, v179 row_ror:8 row_mask:0xf bank_mask:0xc\n"
    "v_cvt_pk_bf16_f32 v237, v222, v223\n"
    "v_mfma_f32_16x16x32_bf16 v[40:43], a[142:145], v[96:99], v[40:43]\n"
    "ds_read_b128 v[96:99], v3 offset:38528\n"
    "v_add_f32_dpp v224, v180, v168 row_ror:8 row_mask:0xf bank_mask:0x3\n"
    "v_mfma_f32_16x16x32_bf16 v[32:35], a[150:153], v[76:79], v[32:35]\n"
    "ds_write_b128 v14, v[234:237] offset:38400\n"
    "v_mfma_f32_16x16x32_bf16 v[36:39], a[158:161], v[76:79], v[36:39]\n"
    "s_waitcnt lgkmcnt(5)\n"
    "v_sub_f32_dpp v224, v168, v180 row_ror:8 row_mask:0xf bank_mask:0xc\n"
    "v_add_f32_dpp v225, v181, v169 row_ror:8 row_mask:0xf bank_mask:0x3\n"
    "v_mfma_f32_16x16x32_bf16 v[40:43], a[166:169], v[76:79], v[40:43]\n"
    "ds_read_b128 v[76:79], v4 offset:38400\n"
    "v_sub_f32_dpp v225, v169, v181 row_ror:8 row_mask:0xf bank_mask:0xc\n"
    "v_mfma_f32_16x16x32_bf16 v[32:35], a[174:177], v[80:83], v[32:35]\n"
    "s_waitcnt lgkmcnt(5)\n"
    "v_cvt_pk_bf16_f32 v238, v224, v225\n"
    "v_add_f32_dpp v226, v182, v170 row_ror:8 row_mask:0xf bank_mask:0x3\n"
    "v_mfma_f32_16x16x32_bf16 v[36:39], a[182:185], v[80:83], v[36:39]\n"
    "v_sub_f32_dpp v226, v170, v182 row_ror:8 row_mask:0xf bank_mask:0xc\n"
    "v_add_f32_dpp v227, v183, v171 row_ror:8 row_mask:0xf bank_mask:0x3\n"
    "v_mfma_f32_16x16x32_bf16 v[40:43], a[190:193], v[80:83], v[40:43]\n"
    "ds_read_b128 v[80:83], v4 offset:38464\n"
    "v_sub_f32_dpp v227, v171, v183 row_ror:8 row_mask:0xf bank_mask:0xc\n"
    "v_mfma_f32_16x16x32_bf16 v[32:35], a[198:201], v[84:87], v[32:35]\n"
    "v_cvt_pk_bf16_f32 v239, v226, v227\n"
    "v_mfma_f32_16x16x32_bf16 v[36:39], a[206:209], v[84:87], v[36:39]\n"
    "ds_write_b64 v15, v[238:239] offset:38400\n"
    "v_mfma_f32_16x16x32_bf16 v[40:43], a[214:217], v[84:87], v[40:43]\n"
    "s_waitcnt lgkmcnt(5)\n"
    "v_mfma_f32_16x16x32_bf16 v[44:47], a[2:5], v[88:91], v[32:35]\n"
    "ds_read_b128 v[84:87], v4 offset:38528\n"
    "s_waitcnt lgkmcnt(5)\n"
    "v_mfma_f32_16x16x32_bf16 v[48:51], a[10:13], v[88:91], v[36:39]\n"
    "ds_write_b128 v16, v[32:35] offset:12288\n"
    "v_mfma_f32_16x16x32_bf16 v[52:55], a[18:21], v[88:91], v[40:43]\n"
    "ds_read_b128 v[88:91], v5 offset:38400\n"
    "v_mfma_f32_16x16x32_bf16 v[44:47], a[26:29], v[92:95], v[44:47]\n"
    "s_waitcnt lgkmcnt(5)\n"
    "v_mfma_f32_16x16x32_bf16 v[48:51], a[34:37], v[92:95], v[48:51]\n"
    "ds_write_b128 v16, v[36:39] offset:13312\n"
    "v_mfma_f32_16x16x32_bf16 v[52:55], a[42:45], v[92:95], v[52:55]\n"
    "s_waitcnt lgkmcnt(5)\n"
    "ds_read_b128 v[92:95], v5 offset:38464\n"
    "v_mfma_f32_16x16x32_bf16 v[44:47], a[50:53], v[96:99], v[44:47]\n"
    "v_mfma_f32_16x16x32_bf16 v[48:51], a[58:61], v[96:99], v[48:51]\n"
    "ds_write_b128 v16, v[40:43] offset:14336\n"
    "v_mfma_f32_16x16x32_bf16 v[52:55], a[66:69], v[96:99], v[52:55]\n"
    "ds_read_b128 v[96:99], v5 offset:38528\n"
    "v_mfma_f32_16x16x32_bf16 v[44:47], a[74:77], v[76:79], v[44:47]\n"
    "s_waitcnt lgkmcnt(6)\n"
    "v_mfma_f32_16x16x32_bf16 v[48:51], a[82:85], v[76:79], v[48:51]\n"
    "v_mfma_f32_16x16x32_bf16 v[52:55], a[90:93], v[76:79], v[52:55]\n"
    "ds_read_b128 v[76:79], v0 offset:41472\n"
    "s_waitcnt lgkmcnt(5)\n"
    "v_mfma_f32_16x16x32_bf16 v[44:47], a[98:101], v[80:83], v[44:47]\n"
    "s_waitcnt lgkmcnt(2)\n"
    "s_barrier\n"
    "v_mfma_f32_16x16x32_bf16 v[48:51], a[106:109], v[80:83], v[48:51]\n"
    "ds_read_b128 v[56:59], v17 offset:12288\n"
    "v_mfma_f32_16x16x32_bf16 v[52:55], a[114:117], v[80:83], v[52:55]\n"
    "ds_read_b128 v[80:83], v0 offset:41536\n"
    "v_mfma_f32_16x16x32_bf16 v[44:47], a[122:125], v[84:87], v[44:47]\n"
    "ds_read_b128 v[60:63], v17 offset:13312\n"
    "v_mfma_f32_16x16x32_bf16 v[48:51], a[130:133], v[84:87], v[48:51]\n"
    "ds_read_b128 v[64:67], v17 offset:14336\n"
    "v_mfma_f32_16x16x32_bf16 v[52:55], a[138:141], v[84:87], v[52:55]\n"
    "ds_read_b128 v[84:87], v0 offset:41600\n"
    "v_mfma_f32_16x16x32_bf16 v[44:47], a[146:149], v[88:91], v[44:47]\n"
    "ds_read_b128 v[252:255], v248 offset:0\n"
    "v_mfma_f32_16x16x32_bf16 v[48:51], a[154:157], v[88:91], v[48:51]\n"
    "ds_read_b128 v[240:243], v249 offset:0\n"
    "s_waitcnt lgkmcnt(8)\n"
    "v_mfma_f32_16x16x32_bf16 v[52:55], a[162:165], v[88:91], v[52:55]\n"
    "ds_read_b128 v[88:91], v1 offset:41472\n"
    "v_mfma_f32_16x16x32_bf16 v[44:47], a[170:173], v[92:95], v[44:47]\n"
    "v_mfma_f32_16x16x32_bf16 v[48:51], a[178:181], v[92:95], v[48:51]\n"
    "s_waitcnt lgkmcnt(8)\n"
    "v_mfma_f32_16x16x32_bf16 v[52:55], a[186:189], v[92:95], v[52:55]\n"
    "ds_read_b128 v[92:95], v1 offset:41536\n"
    "v_mfma_f32_16x16x32_bf16 v[44:47], a[194:197], v[96:99], v[44:47]\n"
    "s_waitcnt lgkmcnt(8)\n"
    "v_mfma_f32_16x16x32_bf16 v[48:51], a[202:205], v[96:99], v[48:51]\n"
    "s_waitcnt lgkmcnt(7)\n"
    "v_mfma_f32_16x16x32_bf16 v[52:55], a[210:213], v[96:99], v[52:55]\n"
    "ds_read_b128 v[96:99], v1 offset:41600\n"
    "s_waitcnt lgkmcnt(7)\n"
    "v_mfma_f32_16x16x32_bf16 v[32:35], a[6:9], v[76:79], 0\n"
    "s_waitcnt lgkmcnt(4)\n"
    "s_add_u32 s53, s47, 0x91800\n"
    "buffer_store_dwordx4 v[252:255], v31, s[24:27], s53 offen offset:0 sc1 nt\n"
    "v_mfma_f32_16x16x32_bf16 v[36:39], a[14:17], v[76:79], 0\n"
    "v_fma_f32 v56, v44, s44, v56\n"
    "v_fma_f32 v57, v45, s44, v57\n"
    "v_mfma_f32_16x16x32_bf16 v[40:43], a[22:25], v[76:79], 0\n"
    "ds_read_b128 v[76:79], v2 offset:41472\n"
    "v_cvt_pk_bf16_f32 v68, v56, v57\n"
    "v_mfma_f32_16x16x32_bf16 v[32:35], a[30:33], v[80:83], v[32:35]\n"
    "s_waitcnt lgkmcnt(3)\n"
    "v_fma_f32 v58, v46, s44, v58\n"
    "v_fma_f32 v59, v47, s44, v59\n"
    "v_mfma_f32_16x16x32_bf16 v[36:39], a[38:41], v[80:83], v[36:39]\n"
    "v_cvt_pk_bf16_f32 v69, v58, v59\n"
    "v_fma_f32 v60, v48, s44, v60\n"
    "v_mfma_f32_16x16x32_bf16 v[40:43], a[46:49], v[80:83], v[40:43]\n"
    "ds_read_b128 v[80:83], v2 offset:41536\n"
    "v_fma_f32 v61, v49, s44, v61\n"
    "v_mfma_f32_16x16x32_bf16 v[32:35], a[54:57], v[84:87], v[32:35]\n"
    "s_add_u32 s54, s47, 0x91800\n"
    "s_mov_b32 exec_hi, 0\n"
    "buffer_store_dwordx4 v[240:243], v31, s[24:27], s54 offen offset:1024 sc1 nt\n"
    "s_mov_b32 exec_hi, -1\n"
    "v_mfma_f32_16x16x32_bf16 v[36:39], a[62:65], v[84:87], v[36:39]\n"
    "s_waitcnt lgkmcnt(3)\n"
    "v_cvt_pk_bf16_f32 v70, v60, v61\n"
    "v_fma_f32 v62, v50, s44, v62\n"
    "v_mfma_f32_16x16x32_bf16 v[40:43], a[70:73], v[84:87], v[40:43]\n"
    "ds_read_b128 v[84:87], v2 offset:41600\n"
    "v_fma_f32 v63, v51, s44, v63\n"
    "v_mfma_f32_16x16x32_bf16 v[32:35], a[78:81], v[88:91], v[32:35]\n"
    "s_waitcnt lgkmcnt(3)\n"
    "v_cvt_pk_bf16_f32 v71, v62, v63\n"
    "v_fma_f32 v64, v52, s44, v64\n"
    "v_mfma_f32_16x16x32_bf16 v[36:39], a[86:89], v[88:91], v[36:39]\n"
    "ds_write_b128 v27, v[68:71] offset:6144\n"
    "v_mfma_f32_16x16x32_bf16 v[40:43], a[94:97], v[88:91], v[40:43]\n"
    "s_waitcnt lgkmcnt(3)\n"
    "ds_read_b128 v[88:91], v3 offset:41472\n"
    "v_mfma_f32_16x16x32_bf16 v[32:35], a[102:105], v[92:95], v[32:35]\n"
    "v_fma_f32 v65, v53, s44, v65\n"
    "v_cvt_pk_bf16_f32 v72, v64, v65\n"
    "v_mfma_f32_16x16x32_bf16 v[36:39], a[110:113], v[92:95], v[36:39]\n"
    "v_fma_f32 v66, v54, s44, v66\n"
    "v_fma_f32 v67, v55, s44, v67\n"
    "v_mfma_f32_16x16x32_bf16 v[40:43], a[118:121], v[92:95], v[40:43]\n"
    "ds_read_b128 v[92:95], v3 offset:41536\n"
    "v_cvt_pk_bf16_f32 v73, v66, v67\n"
    "v_mfma_f32_16x16x32_bf16 v[32:35], a[126:129], v[96:99], v[32:35]\n"
    "ds_write_b64 v28, v[72:73] offset:6144\n"
    "v_mfma_f32_16x16x32_bf16 v[36:39], a[134:137], v[96:99], v[36:39]\n"
    "s_waitcnt lgkmcnt(5)\n"
    "v_mfma_f32_16x16x32_bf16 v[40:43], a[142:145], v[96:99], v[40:43]\n"
    "ds_read_b128 v[96:99], v3 offset:41600\n"
    "v_mfma_f32_16x16x32_bf16 v[32:35], a[150:153], v[76:79], v[32:35]\n"
    "s_waitcnt lgkmcnt(5)\n"
    "v_mfma_f32_16x16x32_bf16 v[36:39], a[158:161], v[76:79], v[36:39]\n"
    "v_mfma_f32_16x16x32_bf16 v[40:43], a[166:169], v[76:79], v[40:43]\n"
    "ds_read_b128 v[76:79], v4 offset:41472\n"
    "v_mfma_f32_16x16x32_bf16 v[32:35], a[174:177], v[80:83], v[32:35]\n"
    "v_mfma_f32_16x16x32_bf16 v[36:39], a[182:185], v[80:83], v[36:39]\n"
    "s_waitcnt lgkmcnt(4)\n"
    "v_mfma_f32_16x16x32_bf16 v[40:43], a[190:193], v[80:83], v[40:43]\n"
    "ds_read_b128 v[80:83], v4 offset:41536\n"
    "v_mfma_f32_16x16x32_bf16 v[32:35], a[198:201], v[84:87], v[32:35]\n"
    "v_mfma_f32_16x16x32_bf16 v[36:39], a[206:209], v[84:87], v[36:39]\n"
    "s_waitcnt lgkmcnt(4)\n"
    "v_mfma_f32_16x16x32_bf16 v[40:43], a[214:217], v[84:87], v[40:43]\n"
    "ds_read_b128 v[84:87], v4 offset:41600\n"
    "v_mfma_f32_16x16x32_bf16 v[44:47], a[2:5], v[88:91], v[32:35]\n"
    "s_nop 2\n"
    "ds_write_b128 v16, v[32:35] offset:0\n"
    "v_mfma_f32_16x16x32_bf16 v[48:51], a[10:13], v[88:91], v[36:39]\n"
    "s_waitcnt lgkmcnt(4)\n"
    "v_mfma_f32_16x16x32_bf16 v[52:55], a[18:21], v[88:91], v[40:43]\n"
    "ds_read_b128 v[88:91], v5 offset:41472\n"
    "v_mfma_f32_16x16x32_bf16 v[44:47], a[26:29], v[92:95], v[44:47]\n"
    "ds_write_b128 v16, v[36:39] offset:1024\n"
    "v_mfma_f32_16x16x32_bf16 v[48:51], a[34:37], v[92:95], v[48:51]\n"
    "s_waitcnt lgkmcnt(5)\n"
    "v_mfma_f32_16x16x32_bf16 v[52:55], a[42:45], v[92:95], v[52:55]\n"
    "ds_read_b128 v[92:95], v5 offset:41536\n"
    "s_waitcnt lgkmcnt(5)\n"
    "v_mfma_f32_16x16x32_bf16 v[44:47], a[50:53], v[96:99], v[44:47]\n"
    "ds_write_b128 v16, v[40:43] offset:2048\n"
    "v_mfma_f32_16x16x32_bf16 v[48:51], a[58:61], v[96:99], v[48:51]\n"
    "v_mfma_f32_16x16x32_bf16 v[52:55], a[66:69], v[96:99], v[52:55]\n"
    "ds_read_b128 v[96:99], v5 offset:41600\n"
    "s_waitcnt lgkmcnt(6)\n"
    "v_mfma_f32_16x16x32_bf16 v[44:47], a[74:77], v[76:79], v[44:47]\n"
    "v_mfma_f32_16x16x32_bf16 v[48:51], a[82:85], v[76:79], v[48:51]\n"
    "v_mfma_f32_16x16x32_bf16 v[52:55], a[90:93], v[76:79], v[52:55]\n"
    "s_waitcnt lgkmcnt(1)\n"
    "s_barrier\n"
    "v_mfma_f32_16x16x32_bf16 v[44:47], a[98:101], v[80:83], v[44:47]\n"
    "ds_read_b128 v[76:79], v0 offset:44544\n"
    "v_mfma_f32_16x16x32_bf16 v[48:51], a[106:109], v[80:83], v[48:51]\n"
    "ds_read_b128 v[56:59], v17 offset:0\n"
    "v_mfma_f32_16x16x32_bf16 v[52:55], a[114:117], v[80:83], v[52:55]\n"
    "ds_read_b128 v[80:83], v0 offset:44608\n"
    "v_mfma_f32_16x16x32_bf16 v[44:47], a[122:125], v[84:87], v[44:47]\n"
    "ds_read_b128 v[60:63], v17 offset:1024\n"
    "v_mfma_f32_16x16x32_bf16 v[48:51], a[130:133], v[84:87], v[48:51]\n"
    "ds_read_b128 v[64:67], v17 offset:2048\n"
    "v_mfma_f32_16x16x32_bf16 v[52:55], a[138:141], v[84:87], v[52:55]\n"
    "ds_read_b128 v[84:87], v0 offset:44672\n"
    "v_mfma_f32_16x16x32_bf16 v[44:47], a[146:149], v[88:91], v[44:47]\n"
    "ds_read_b128 v[252:255], v248 offset:6144\n"
    "s_waitcnt lgkmcnt(7)\n"
    "v_mfma_f32_16x16x32_bf16 v[48:51], a[154:157], v[88:91], v[48:51]\n"
    "ds_read_b128 v[240:243], v249 offset:6144\n"
    "v_mfma_f32_16x16x32_bf16 v[52:55], a[162:165], v[88:91], v[52:55]\n"
    "ds_read_b128 v[88:91], v1 offset:44544\n"
    "v_mfma_f32_16x16x32_bf16 v[44:47], a[170:173], v[92:95], v[44:47]\n"
    "v_mfma_f32_16x16x32_bf16 v[48:51], a[178:181], v[92:95], v[48:51]\n"
    "v_mfma_f32_16x16x32_bf16 v[52:55], a[186:189], v[92:95], v[52:55]\n"
    "ds_read_b128 v[92:95], v1 offset:44608\n"
    "s_waitcnt lgkmcnt(9)\n"
    "v_mfma_f32_16x16x32_bf16 v[44:47], a[194:197], v[96:99], v[44:47]\n"
    "s_waitcnt lgkmcnt(8)\n"
    "v_mfma_f32_16x16x32_bf16 v[48:51], a[202:205], v[96:99], v[48:51]\n"
    "s_waitcnt lgkmcnt(7)\n"
    "v_mfma_f32_16x16x32_bf16 v[52:55], a[210:213], v[96:99], v[52:55]\n"
    "ds_read_b128 v[96:99], v1 offset:44672\n"
    "s_waitcnt lgkmcnt(7)\n"
    "v_mfma_f32_16x16x32_bf16 v[32:35], a[6:9], v[76:79], 0\n"
    "s_waitcnt lgkmcnt(4)\n"
    "s_add_u32 s55, s47, 0xb4000\n"
    "buffer_store_dwordx4 v[252:255], v31, s[24:27], s55 offen offset:0 sc1 nt\n"
    "v_mfma_f32_16x16x32_bf16 v[36:39], a[14:17], v[76:79], 0\n"
    "v_fma_f32 v56, v44, s44, v56\n"
    "v_fma_f32 v57, v45, s44, v57\n"
    "v_mfma_f32_16x16x32_bf16 v[40:43], a[22:25], v[76:79], 0\n"
    "ds_read_b128 v[76:79], v2 offset:44544\n"
    "v_cvt_pk_bf16_f32 v68, v56, v57\n"
    "v_mfma_f32_16x16x32_bf16 v[32:35], a[30:33], v[80:83], v[32:35]\n"
    "s_waitcnt lgkmcnt(3)\n"
    "v_fma_f32 v58, v46, s44, v58\n"
    "v_fma_f32 v59, v47, s44, v59\n"
    "v_mfma_f32_16x16x32_bf16 v[36:39], a[38:41], v[80:83], v[36:39]\n"
    "v_cvt_pk_bf16_f32 v69, v58, v59\n"
    "v_fma_f32 v60, v48, s44, v60\n"
    "v_mfma_f32_16x16x32_bf16 v[40:43], a[46:49], v[80:83], v[40:43]\n"
    "ds_read_b128 v[80:83], v2 offset:44608\n"
    "v_fma_f32 v61, v49, s44, v61\n"
    "v_mfma_f32_16x16x32_bf16 v[32:35], a[54:57], v[84:87], v[32:35]\n"
    "s_add_u32 s52, s47, 0xb4000\n"
    "s_mov_b32 exec_hi, 0\n"
    "buffer_store_dwordx4 v[240:243], v31, s[24:27], s52 offen offset:1024 sc1 nt\n"
    "s_mov_b32 exec_hi, -1\n"
    "v_mfma_f32_16x16x32_bf16 v[36:39], a[62:65], v[84:87], v[36:39]\n"
    "s_waitcnt lgkmcnt(3)\n"
    "v_cvt_pk_bf16_f32 v70, v60, v61\n"
    "v_fma_f32 v62, v50, s44, v62\n"
    "v_mfma_f32_16x16x32_bf16 v[40:43], a[70:73], v[84:87], v[40:43]\n"
    "ds_read_b128 v[84:87], v2 offset:44672\n"
    "v_fma_f32 v63, v51, s44, v63\n"
    "v_mfma_f32_16x16x32_bf16 v[32:35], a[78:81], v[88:91], v[32:35]\n"
    "s_waitcnt lgkmcnt(3)\n"
    "v_cvt_pk_bf16_f32 v71, v62, v63\n"
    "v_fma_f32 v64, v52, s44, v64\n"
    "v_mfma_f32_16x16x32_bf16 v[36:39], a[86:89], v[88:91], v[36:39]\n"
    "ds_write_b128 v27, v[68:71] offset:0\n"
    "v_mfma_f32_16x16x32_bf16 v[40:43], a[94:97], v[88:91], v[40:43]\n"
    "s_waitcnt lgkmcnt(3)\n"
    "ds_read_b128 v[88:91], v3 offset:44544\n"
    "v_mfma_f32_16x16x32_bf16 v[32:35], a[102:105], v[92:95], v[32:35]\n"
    "v_fma_f32 v65, v53, s44, v65\n"
    "v_cvt_pk_bf16_f32 v72, v64, v65\n"
    "v_mfma_f32_16x16x32_bf16 v[36:39], a[110:113], v[92:95], v[36:39]\n"
    "v_fma_f32 v66, v54, s44, v66\n"
    "v_fma_f32 v67, v55, s44, v67\n"
    "v_mfma_f32_16x16x32_bf16 v[40:43], a[118:121], v[92:95], v[40:43]\n"
    "ds_read_b128 v[92:95], v3 offset:44608\n"
    "v_cvt_pk_bf16_f32 v73, v66, v67\n"
    "v_mfma_f32_16x16x32_bf16 v[32:35], a[126:129], v[96:99], v[32:35]\n"
    "ds_write_b64 v28, v[72:73] offset:0\n"
    "v_mfma_f32_16x16x32_bf16 v[36:39], a[134:137], v[96:99], v[36:39]\n"
    "s_waitcnt lgkmcnt(5)\n"
    "v_mfma_f32_16x16x32_bf16 v[40:43], a[142:145], v[96:99], v[40:43]\n"
    "ds_read_b128 v[96:99], v3 offset:44672\n"
    "v_mfma_f32_16x16x32_bf16 v[32:35], a[150:153], v[76:79], v[32:35]\n"
    "s_waitcnt lgkmcnt(5)\n"
    "v_mfma_f32_16x16x32_bf16 v[36:39], a[158:161], v[76:79], v[36:39]\n"
    "v_mfma_f32_16x16x32_bf16 v[40:43], a[166:169], v[76:79], v[40:43]\n"
    "ds_read_b128 v[76:79], v4 offset:44544\n"
    "v_mfma_f32_16x16x32_bf16 v[32:35], a[174:177], v[80:83], v[32:35]\n"
    "v_mfma_f32_16x16x32_bf16 v[36:39], a[182:185], v[80:83], v[36:39]\n"
    "s_waitcnt lgkmcnt(4)\n"
    "v_mfma_f32_16x16x32_bf16 v[40:43], a[190:193], v[80:83], v[40:43]\n"
    "ds_read_b128 v[80:83], v4 offset:44608\n"
    "v_mfma_f32_16x16x32_bf16 v[32:35], a[198:201], v[84:87], v[32:35]\n"
    "v_mfma_f32_16x16x32_bf16 v[36:39], a[206:209], v[84:87], v[36:39]\n"
    "s_waitcnt lgkmcnt(4)\n"
    "v_mfma_f32_16x16x32_bf16 v[40:43], a[214:217], v[84:87], v[40:43]\n"
    "ds_read_b128 v[84:87], v4 offset:44672\n"
    "v_mfma_f32_16x16x32_bf16 v[44:47], a[2:5], v[88:91], v[32:35]\n"
    "s_nop 2\n"
    "ds_write_b128 v16, v[32:35] offset:12288\n"
    "v_mfma_f32_16x16x32_bf16 v[48:51], a[10:13], v[88:91], v[36:39]\n"
    "s_waitcnt lgkmcnt(4)\n"
    "v_mfma_f32_16x16x32_bf16 v[52:55], a[18:21], v[88:91], v[40:43]\n"
    "ds_read_b128 v[88:91], v5 offset:44544\n"
    "v_mfma_f32_16x16x32_bf16 v[44:47], a[26:29], v[92:95], v[44:47]\n"
    "ds_write_b128 v16, v[36:39] offset:13312\n"
    "v_mfma_f32_16x16x32_bf16 v[48:51], a[34:37], v[92:95], v[48:51]\n"
    "s_waitcnt lgkmcnt(5)\n"
    "v_mfma_f32_16x16x32_bf16 v[52:55], a[42:45], v[92:95], v[52:55]\n"
    "ds_read_b128 v[92:95], v5 offset:44608\n"
    "s_waitcnt lgkmcnt(5)\n"
    "v_mfma_f32_16x16x32_bf16 v[44:47], a[50:53], v[96:99], v[44:47]\n"
    "ds_write_b128 v16, v[40:43] offset:14336\n"
    "v_mfma_f32_16x16x32_bf16 v[48:51], a[58:61], v[96:99], v[48:51]\n"
    "v_mfma_f32_16x16x32_bf16 v[52:55], a[66:69], v[96:99], v[52:55]\n"
    "ds_read_b128 v[96:99], v5 offset:44672\n"
    "s_waitcnt lgkmcnt(6)\n"
    "v_mfma_f32_16x16x32_bf16 v[44:47], a[74:77], v[76:79], v[44:47]\n"
    "v_mfma_f32_16x16x32_bf16 v[48:51], a[82:85], v[76:79], v[48:51]\n"
    "v_mfma_f32_16x16x32_bf16 v[52:55], a[90:93], v[76:79], v[52:55]\n"
    "s_waitcnt lgkmcnt(1)\n"
    "s_barrier\n"
    "v_mfma_f32_16x16x32_bf16 v[44:47], a[98:101], v[80:83], v[44:47]\n"
    "ds_read_b128 v[56:59], v17 offset:12288\n"
    "v_mfma_f32_16x16x32_bf16 v[48:51], a[106:109], v[80:83], v[48:51]\n"
    "ds_read_b128 v[60:63], v17 offset:13312\n"
    "v_mfma_f32_16x16x32_bf16 v[52:55], a[114:117], v[80:83], v[52:55]\n"
    "ds_read_b128 v[64:67], v17 offset:14336\n"
    "v_mfma_f32_16x16x32_bf16 v[44:47], a[122:125], v[84:87], v[44:47]\n"
    "ds_read_b128 v[252:255], v248 offset:0\n"
    "v_mfma_f32_16x16x32_bf16 v[48:51], a[130:133], v[84:87], v[48:51]\n"
    "ds_read_b128 v[240:243], v249 offset:0\n"
    "v_mfma_f32_16x16x32_bf16 v[52:55], a[138:141], v[84:87], v[52:55]\n"
    "v_mfma_f32_16x16x32_bf16 v[44:47], a[146:149], v[88:91], v[44:47]\n"
    "v_mfma_f32_16x16x32_bf16 v[48:51], a[154:157], v[88:91], v[48:51]\n"
    "s_waitcnt lgkmcnt(5)\n"
    "v_mfma_f32_16x16x32_bf16 v[52:55], a[162:165], v[88:91], v[52:55]\n"
    "v_mfma_f32_16x16x32_bf16 v[44:47], a[170:173], v[92:95], v[44:47]\n"
    "v_mfma_f32_16x16x32_bf16 v[48:51], a[178:181], v[92:95], v[48:51]\n"
    "v_mfma_f32_16x16x32_bf16 v[52:55], a[186:189], v[92:95], v[52:55]\n"
    "s_waitcnt lgkmcnt(4)\n"
    "v_mfma_f32_16x16x32_bf16 v[44:47], a[194:197], v[96:99], v[44:47]\n"
    "s_waitcnt lgkmcnt(1)\n"
    "s_add_u32 s53, s47, 0xb4c00\n"
    "buffer_store_dwordx4 v[252:255], v31, s[24:27], s53 offen offset:0 sc1 nt\n"
    "v_mfma_f32_16x16x32_bf16 v[48:51], a[202:205], v[96:99], v[48:51]\n"
    "v_mfma_f32_16x16x32_bf16 v[52:55], a[210:213], v[96:99], v[52:55]\n"
    "s_waitcnt lgkmcnt(0)\n"
    "s_nop 1\n"
    "v_fma_f32 v56, v44, s44, v56\n"
    "v_fma_f32 v57, v45, s44, v57\n"
    "v_cvt_pk_bf16_f32 v68, v56, v57\n"
    "v_fma_f32 v58, v46, s44, v58\n"
    "v_fma_f32 v59, v47, s44, v59\n"
    "v_cvt_pk_bf16_f32 v69, v58, v59\n"
    "v_fma_f32 v60, v48, s44, v60\n"
    "v_fma_f32 v61, v49, s44, v61\n"
    "v_cvt_pk_bf16_f32 v70, v60, v61\n"
    "s_add_u32 s54, s47, 0xb4c00\n"
    "s_mov_b32 exec_hi, 0\n"
    "buffer_store_dwordx4 v[240:243], v31, s[24:27], s54 offen offset:1024 sc1 nt\n"
    "s_mov_b32 exec_hi, -1\n"
    "v_fma_f32 v62, v50, s44, v62\n"
    "v_fma_f32 v63, v51, s44, v63\n"
    "v_cvt_pk_bf16_f32 v71, v62, v63\n"
    "ds_write_b128 v27, v[68:71] offset:6144\n"
    "v_fma_f32 v64, v52, s44, v64\n"
    "v_fma_f32 v65, v53, s44, v65\n"
    "v_cvt_pk_bf16_f32 v72, v64, v65\n"
    "v_fma_f32 v66, v54, s44, v66\n"
    "v_fma_f32 v67, v55, s44, v67\n"
    "v_cvt_pk_bf16_f32 v73, v66, v67\n"
    "ds_write_b64 v28, v[72:73] offset:6144\n"
    "s_waitcnt lgkmcnt(0)\n"
    "s_barrier\n"
    "ds_read_b128 v[252:255], v248 offset:6144\n"
    "ds_read_b128 v[240:243], v249 offset:6144\n"
    "s_waitcnt lgkmcnt(1)\n"
    "s_add_u32 s55, s47, 0xb5800\n"
    "buffer_store_dwordx4 v[252:255], v31, s[24:27], s55 offen offset:0 sc1 nt\n"
    "s_waitcnt lgkmcnt(0)\n"
    "s_add_u32 s52, s47, 0xb5800\n"
    "s_mov_b32 exec_hi, 0\n"
    "buffer_store_dwordx4 v[240:243], v31, s[24:27], s52 offen offset:1024 sc1 nt\n"
    "s_mov_b32 exec_hi, -1\n"
    ".LvE:\n"
    : "+{v0}"(tid), "+{s[0:1]}"(ka), "+{s2}"(bid)
    :
    : "memory", "vcc", "scc", "v1", "v2", "v3", "v4", "v5", "v6", "v7", "v8", "v9", "v10", "v11", "v12", "v13", "v14", "v15", "v16", "v17", "v18", "v19", "v20", "v21", "v22", "v23", "v24", "v25", "v26", "v27", "v28", "v29", "v30", "v31", "v32", "v33", "v34", "v35", "v36", "v37", "v38", "v39", "v40", "v41", "v42", "v43", "v44", "v45", "v46", "v47", "v48", "v49", "v50", "v51", "v52", "v53", "v54", "v55", "v56", "v57", "v58", "v59", "v60", "v61", "v62", "v63", "v64", "v65", "v66", "v67", "v68", "v69", "v70", "v71", "v72", "v73", "v74", "v75", "v76", "v77", "v78", "v79", "v80", "v81", "v82", "v83", "v84", "v85", "v86", "v87", "v88", "v89", "v90", "v91", "v92", "v93", "v94", "v95", "v96", "v97", "v98", "v99", "v100", "v101", "v102", "v103", "v104", "v105", "v106", "v107", "v108", "v109", "v110", "v111", "v112", "v113", "v114", "v115", "v116", "v117", "v118", "v119", "v120", "v121", "v122", "v123", "v124", "v125", "v126", "v127", "v128", "v129", "v130", "v131", "v132", "v133", "v134", "v135", "v136", "v137", "v138", "v139", "v140", "v141", "v142", "v143", "v144", "v145", "v146", "v147", "v148", "v149", "v150", "v151", "v152", "v153", "v154", "v155", "v156", "v157", "v158", "v159", "v160", "v161", "v162", "v163", "v164", "v165", "v166", "v167", "v168", "v169", "v170", "v171", "v172", "v173", "v174", "v175", "v176", "v177", "v178", "v179", "v180", "v181", "v182", "v183", "v184", "v185", "v186", "v187", "v188", "v189", "v190", "v191", "v192", "v193", "v194", "v195", "v196", "v197", "v198", "v199", "v200", "v201", "v202", "v203", "v204", "v205", "v206", "v207", "v208", "v209", "v210", "v211", "v212", "v213", "v214", "v215", "v216", "v217", "v218", "v219", "v220", "v221", "v222", "v223", "v224", "v225", "v226", "v227", "v228", "v229", "v230", "v231", "v232", "v233", "v234", "v235", "v236", "v237", "v238", "v239", "v240", "v241", "v242", "v243", "v244", "v245", "v246", "v247", "v248", "v249", "v250", "v251", "v252", "v253", "v254", "v255", "a0", "a1", "a2", "a3", "a4", "a5", "a6", "a7", "a8", "a9", "a10", "a11", "a12", "a13", "a14", "a15", "a16", "a17", "a18", "a19", "a20", "a21", "a22", "a23", "a24", "a25", "a26", "a27", "a28", "a29", "a30", "a31", "a32", "a33", "a34", "a35", "a36", "a37", "a38", "a39", "a40", "a41", "a42", "a43", "a44", "a45", "a46", "a47", "a48", "a49", "a50", "a51", "a52", "a53", "a54", "a55", "a56", "a57", "a58", "a59", "a60", "a61", "a62", "a63", "a64", "a65", "a66", "a67", "a68", "a69", "a70", "a71", "a72", "a73", "a74", "a75", "a76", "a77", "a78", "a79", "a80", "a81", "a82", "a83", "a84", "a85", "a86", "a87", "a88", "a89", "a90", "a91", "a92", "a93", "a94", "a95", "a96", "a97", "a98", "a99", "a100", "a101", "a102", "a103", "a104", "a105", "a106", "a107", "a108", "a109", "a110", "a111", "a112", "a113", "a114", "a115", "a116", "a117", "a118", "a119", "a120", "a121", "a122", "a123", "a124", "a125", "a126", "a127", "a128", "a129", "a130", "a131", "a132", "a133", "a134", "a135", "a136", "a137", "a138", "a139", "a140", "a141", "a142", "a143", "a144", "a145", "a146", "a147", "a148", "a149", "a150", "a151", "a152", "a153", "a154", "a155", "a156", "a157", "a158", "a159", "a160", "a161", "a162", "a163", "a164", "a165", "a166", "a167", "a168", "a169", "a170", "a171", "a172", "a173", "a174", "a175", "a176", "a177", "a178", "a179", "a180", "a181", "a182", "a183", "a184", "a185", "a186", "a187", "a188", "a189", "a190", "a191", "a192", "a193", "a194", "a195", "a196", "a197", "a198", "a199", "a200", "a201", "a202", "a203", "a204", "a205", "a206", "a207", "a208", "a209", "a210", "a211", "a212", "a213", "a214", "a215", "a216", "a217", "a218", "a219", "a220", "a221", "a222", "a223", "a224", "a225", "a226", "a227", "a228", "a229", "a230", "a231", "a232", "a233", "a234", "a235", "a236", "a237", "a238", "a239", "a240", "a241", "a242", "a243", "a244", "a245", "a246", "a247", "a248", "a249", "a250", "a251", "a252", "a253", "a254", "a255", "s3", "s4", "s5", "s6", "s7", "s8", "s9", "s10", "s11", "s12", "s13", "s14", "s15", "s16", "s17", "s18", "s19", "s20", "s21", "s22", "s23", "s24", "s25", "s26", "s27", "s28", "s29", "s30", "s31", "s33", "s34", "s35", "s36", "s37", "s38", "s39", "s40", "s41", "s42", "s43", "s44", "s45", "s46", "s47", "s48", "s49", "s50", "s51", "s52", "s53", "s54", "s55", "s56", "s57", "s58", "s59", "s60", "s61", "s62", "s63", "s64", "s65", "s66", "s67", "s68", "s69", "s70", "s71", "s72", "s73", "s74", "s75", "s76", "s77", "s78", "s79", "s80", "s81", "s82", "s83", "s84", "s85", "s86", "s87", "s90", "s91", "s94", "s95", "s96", "s97", "s98", "s99");
}

extern "C" void kernel_launch(void* const* d_in, const int* in_sizes, int n_in,
                              void* d_out, int out_size, void* d_ws, size_t ws_size,
                              hipStream_t stream) {
  fpn_lateral_conv_kernel<<<256, 256, 0, stream>>>(d_in[0], d_in[1], d_in[2], d_in[3], d_out);
}
```
